```python
import torch

def _softmax(x, axis=-1):
    return torch.softmax(x, dim=axis)

def _gelu(x):
    return torch.nn.functional.gelu(x, approximate='tanh')

def _silu(x):
    return torch.nn.functional.silu(x)

def _sigmoid(x):
    return torch.sigmoid(x)

def _layernorm(x, axis=-1, eps=1e-05):
    m = x.mean(dim=axis, keepdim=True)
    v = x.var(dim=axis, keepdim=True, unbiased=False)
    return (x - m) / torch.sqrt(v + eps)

def _scatter_add(tgt, idx, src, axis=0):
    return tgt.clone().index_add_(axis, idx, src)

def _segment_sum(src, seg, n):
    out = torch.zeros((n,) + src.shape[1:], dtype=src.dtype)
    return out.index_add_(0, seg, src)

def _pad(x, pw):
    flat = tuple((v for pair in reversed(pw) for v in pair))
    return torch.nn.functional.pad(x, flat)
kernel_name = 'recurrent_scan__sliding_block__mixer_gmlp__prompt'

def setup_inputs():

    _w = lambda a: (lambda t: t.to(torch.bfloat16) if t.is_floating_point() else t)(torch.as_tensor(a))

    def _A_setup():
        B, S, D = (1, 2048, 576)
        return {'x': _w((lambda *s: torch.randn(*s, dtype=torch.bfloat16) * 0.05)(B, S, D)), 'wqkv': _w((lambda *s: torch.randn(*s, dtype=torch.bfloat16) * 0.05)(D, 3 * D)), 'wo': _w((lambda *s: torch.randn(*s, dtype=torch.bfloat16) * 0.05)(D, D))}
    _A_in = _A_setup()

    def _B_setup():
        B, S, D, F = (1, 2048, 576, 4608)
        Fh = F // 2
        return {'x': _w((lambda *s: torch.randn(*s, dtype=torch.bfloat16) * 0.05)(B, S, D)), 'w_in': _w((lambda *s: torch.randn(*s, dtype=torch.bfloat16) * 0.4)(D, 2 * Fh)), 'w_sp': _w(0.01 * (lambda *s: torch.randn(*s, dtype=torch.bfloat16) * 0.4)(S, S)), 'b_sp': _w(1.0 + 0.01 * (lambda *s: torch.randn(*s, dtype=torch.bfloat16) * 0.05)(S)), 'w_out': _w((lambda *s: torch.randn(*s, dtype=torch.bfloat16) * 0.05)(Fh, D))}
    _B_in = _B_setup()
    return {'A_x': _A_in['x'], 'A_wqkv': _A_in['wqkv'], 'A_wo': _A_in['wo'], 'B_x': _B_in['x'], 'B_w_in': _B_in['w_in'], 'B_w_sp': _B_in['w_sp'], 'B_b_sp': _B_in['b_sp'], 'B_w_out': _B_in['w_out']}

def reference(A_x, A_wqkv, A_wo, B_x, B_w_in, B_w_sp, B_b_sp, B_w_out):

    def _A_ref(x, wqkv, wo):
        B, S, D = x.shape
        H = 32
        Dh = D // H
        W = 256
        qkv = (lambda x, *d: x.permute(*d))((x @ wqkv).reshape(B, S, 3, H, Dh), 2, 0, 3, 1, 4)
        q, k, v = (qkv[0], qkv[1], qkv[2])
        scores = torch.einsum('bhqd,bhkd->bhqk', q, k) * Dh ** (-0.5)
        idx = torch.arange(S)
        mask = (idx[:, None] - idx[None, :] >= 0) & (idx[:, None] - idx[None, :] < W)
        scores = torch.where(mask[None, None], scores, -1000000000.0)
        p = _softmax(scores, axis=-1)
        o = torch.einsum('bhqk,bhkd->bhqd', p, v)
        return (lambda x, *d: x.permute(*d))(o, 0, 2, 1, 3).reshape(B, S, D) @ wo

    def _B_ref(x, w_in, w_sp, b_sp, w_out):
        B, S, D = x.shape
        F = w_in.shape[-1]
        Fh = F // 2
        z = _gelu(torch.einsum('bsd,df->bsf', x, w_in))
        z1 = z[..., :Fh].clone()
        z2 = _layernorm(z[..., Fh:], axis=-1)
        g = torch.einsum('ts,bsf->btf', w_sp, z2) + b_sp[None, :, None]
        return torch.einsum('bsf,fd->bsd', z1 * g, w_out)
    x = A_x
    xn = _layernorm(x, axis=-1)
    s = _A_ref(xn, A_wqkv, A_wo)
    g = _silu(_B_ref(xn, B_w_in, B_w_sp, B_b_sp, B_w_out))
    return x + s * g
```

```cpp
#include <hip/hip_runtime.h>
#include <hip/hip_bf16.h>
#include <stdint.h>
#include <type_traits>

typedef unsigned int u32;
typedef unsigned short u16;
typedef __attribute__((ext_vector_type(8))) __bf16 bf16x8;
typedef __attribute__((ext_vector_type(2))) __bf16 bf16x2;
typedef __attribute__((ext_vector_type(4))) short s16x4;
typedef __attribute__((ext_vector_type(2))) float f32x2;
typedef __attribute__((ext_vector_type(4))) float f32x4;
typedef __attribute__((ext_vector_type(16))) float f32x16;
typedef __attribute__((ext_vector_type(2))) u32 u32x2;
typedef __attribute__((ext_vector_type(4))) u32 u32x4;

#define DEVI __device__ __forceinline__
#define AS1 __attribute__((address_space(1)))
#define AS3 __attribute__((address_space(3)))
#define GLDS(g, l) __builtin_amdgcn_global_load_lds((const AS1 void*)(g), (AS3 void*)(l), 16, 0, 0)
#define WAIT_V(n) asm volatile("s_waitcnt vmcnt(" #n ")" ::: "memory")
#define WAIT_L0() asm volatile("s_waitcnt lgkmcnt(0)" ::: "memory")
#define BAR() do { WAIT_L0(); __builtin_amdgcn_s_barrier(); asm volatile("" ::: "memory"); } while (0)

constexpr int S_ = 2048, D_ = 576, FH = 2304, NZQ = 4032;
constexpr size_t OFF_Z1 = 0;
constexpr size_t OFF_ZQ = OFF_Z1 + (size_t)288 * 2048 * 16;
constexpr size_t OFF_ZN = OFF_ZQ + (size_t)256 * NZQ * 16;
constexpr size_t OFF_PO = OFF_ZN + (size_t)256 * FH * 16;
constexpr size_t OFF_WT = OFF_PO + (size_t)360 * 2048 * 16;
constexpr size_t WS_END = OFF_WT + (size_t)360 * 576 * 16;

DEVI float bf_lo(u32 v) { return __builtin_bit_cast(float, v << 16); }
DEVI float bf_hi(u32 v) { return __builtin_bit_cast(float, v & 0xffff0000u); }
DEVI u32 pk_bf16(float a, float b) {
  f32x2 v = {a, b};
  return __builtin_bit_cast(u32, __builtin_convertvector(v, bf16x2));
}
DEVI f32x4 mfma16(u32x4 a, u32x4 b, f32x4 c) {
  return __builtin_amdgcn_mfma_f32_16x16x32_bf16(__builtin_bit_cast(bf16x8, a), __builtin_bit_cast(bf16x8, b), c, 0, 0, 0);
}
DEVI f32x16 mfma32(u32x4 a, u32x4 b, f32x16 c) {
  return __builtin_amdgcn_mfma_f32_32x32x16_bf16(__builtin_bit_cast(bf16x8, a), __builtin_bit_cast(bf16x8, b), c, 0, 0, 0);
}
DEVI u32x2 tr_read(const char* p) {
  s16x4 v = __builtin_amdgcn_ds_read_tr16_b64_v4i16((AS3 s16x4*)p);
  return __builtin_bit_cast(u32x2, v);
}
DEVI u32x4 tr_read2(const char* p0, const char* p1) {
  u32x2 a = tr_read(p0), b = tr_read(p1);
  u32x4 r = {a[0], a[1], b[0], b[1]};
  return r;
}
DEVI float fexp2(float x) { return __builtin_amdgcn_exp2f(x); }
DEVI float frcp(float x) { return __builtin_amdgcn_rcpf(x); }
DEVI float gelu_tanh(float v) {
  const float c0 = -2.0f * 0.7978845608028654f * 1.4426950408889634f;
  const float c1 = c0 * 0.044715f;
  float a = __builtin_fmaf(v * v, c1, c0);
  float e = fexp2(v * a);
  return v * frcp(1.0f + e);
}
DEVI float silu_f(float v) { return v * frcp(1.0f + fexp2(v * -1.4426950408889634f)); }
DEVI int swap23(int i) { return (i & ~12) | ((i & 4) << 1) | ((i & 8) >> 1); }

constexpr int K0_SLOT = 24576;

template <int ZORI>
DEVI void k0_body(char* lds, const u16* __restrict__ x, const u16* __restrict__ wqkv, const u16* __restrict__ w_in,
                  char* __restrict__ ws, int rt, int cg) {
  const int tid = threadIdx.x, lane = tid & 63;
  const int w = __builtin_amdgcn_readfirstlane(tid >> 6);
  const int tgp = w & 3, cs = w >> 2;
  const int h = lane >> 5;
  const int t0 = rt * 128;
  const int tok = swap23(lane & 31);

  {
    const char* xb = (const char*)x + (size_t)t0 * 1152;
#pragma unroll
    for (int th = 0; th < 3; ++th) {
#pragma unroll
      for (int n = 0; n < 6; ++n) {
        int i = (w * 6 + n) * 64 + lane;
        int row = i / 24, cp = i - row * 24;
        int c = (cp & ~7) | ((cp & 7) ^ ((row >> 1) & 7));
        GLDS(xb + row * 1152 + th * 384 + c * 16, lds + th * 49152 + (w * 6 + n) * 1024);
      }
    }
  }
  const bool is_q = cg >= 12;
  const int npair = (cg >= 6 && cg < 12) ? 7 : 6;
  const int nst = npair * 3;
  const int wrow = 24 * w + (lane >> 3);
  const int wch = (lane & 7) ^ (((lane >> 4) & 1) << 2);
  const char* wsrc_main = is_q ? (const char*)wqkv + (size_t)wrow * 3456 + (cg - 12) * 768 + wch * 16
                               : (const char*)w_in + (size_t)wrow * 9216 + cg * 768 + wch * 16;
  const int wstride_main = is_q ? 3456 : 9216;
  const int single = 48 + (cg - 6);
  const char* wsrc_single = (const char*)wqkv + (size_t)wrow * 3456 + (single >> 1) * 128 + wch * 16;
  auto issue = [&](int q) {
    int p = q / 3, th = q - p * 3;
    const char* src;
    int stride;
    if (p < 6) { src = wsrc_main + p * 128; stride = wstride_main; }
    else       { src = wsrc_single; stride = 3456; }
    src += (size_t)(th * 192) * stride;
    char* dst = lds + (q % 6) * K0_SLOT + w * 3072;
    GLDS(src, dst);
    GLDS(src + 8 * stride, dst + 1024);
    GLDS(src + 16 * stride, dst + 2048);
  };

  u32x4 xa[36];
  {
    const int row = 32 * tgp + tok;
    const int f = (row >> 1) & 7;
    const char* rb = lds + row * 384;
#define RDX(TH)                                                                          \
    _Pragma("unroll") for (int s = 0; s < 12; ++s) {                                     \
      int c = 2 * s + h;                                                                 \
      int cp = (c & ~7) | ((c & 7) ^ f);                                                 \
      xa[TH * 12 + s] = *(const u32x4*)(rb + TH * 49152 + cp * 16);                      \
    }
    WAIT_V(12); BAR();
    RDX(0)
    WAIT_V(6); BAR();
    issue(0); issue(1);
    RDX(1)
    WAIT_V(6); BAR();
    issue(2); issue(3);
    RDX(2)
    BAR();
    issue(4);
#undef RDX
  }
  {
    float s1 = 0.f, s2 = 0.f;
    const bf16x2 ones = __builtin_bit_cast(bf16x2, 0x3f803f80u);
#pragma unroll
    for (int i = 0; i < 36; ++i) {
#pragma unroll
      for (int j = 0; j < 4; ++j) {
        const u32 xv = xa[i][j];
        bf16x2 v = __builtin_bit_cast(bf16x2, xv);
        s1 = __builtin_amdgcn_fdot2_f32_bf16(v, ones, s1, false);
        s2 = __builtin_amdgcn_fdot2_f32_bf16(v, v, s2, false);
      }
    }
    s1 += __shfl_xor(s1, 32);
    s2 += __shfl_xor(s2, 32);
    float mean = s1 * (1.0f / 576.0f);
    float var = fmaxf(s2 * (1.0f / 576.0f) - mean * mean, 0.f);
    float rstd = 1.0f / sqrtf(var + 1e-5f);
    float nb = -mean * rstd;
#pragma unroll
    for (int i = 0; i < 36; ++i) {
#pragma unroll
      for (int j = 0; j < 4; ++j) {
        u32 v = xa[i][j];
        xa[i][j] = pk_bf16(__builtin_fmaf(bf_lo(v), rstd, nb), __builtin_fmaf(bf_hi(v), rstd, nb));
      }
    }
  }

  const int q4 = (lane & 15) >> 2, p4 = lane & 3, g16 = (lane >> 4) & 1;
  const int pp = ZORI ? (((p4 & 1) << 1) | (p4 >> 1)) : p4;
  const int rd_off = (8 * h + q4) * 128 + (((4 * cs + 2 * g16 + (pp >> 1)) ^ ((q4 >> 1) << 2)) * 16) + (pp & 1) * 8;

  for (int p = 0; p < npair; ++p) {
    f32x16 acc;
#pragma unroll
    for (int i = 0; i < 16; ++i) acc[i] = 0.f;
    const bool active = (p < 6) || ((single & 1) == cs);
#pragma unroll
    for (int th = 0; th < 3; ++th) {
      const int q = p * 3 + th;
      const int newer = nst - 1 - q;
      if (newer >= 4) WAIT_V(12);
      else if (newer == 3) WAIT_V(9);
      else if (newer == 2) WAIT_V(6);
      else if (newer == 1) WAIT_V(3);
      else WAIT_V(0);
      BAR();
      if (q + 5 < nst) issue(q + 5);
      if (active) {
        const char* sb = lds + (q % 6) * K0_SLOT + rd_off;
#pragma unroll
        for (int s = 0; s < 12; ++s) {
          u32x4 wf = tr_read2(sb + s * 2048, sb + s * 2048 + 512);
          if (ZORI) acc = mfma32(wf, xa[th * 12 + s], acc);
          else      acc = mfma32(xa[th * 12 + s], wf, acc);
        }
      }
    }
    if (active) {
      if (ZORI) {
        const int fc0 = (cg * 384 + p * 64 + cs * 32) / 8 + h;
        char* dst = ws + OFF_Z1 + ((size_t)fc0 * 2048 + t0 + 32 * tgp + tok) * 16;
#pragma unroll
        for (int u = 0; u < 2; ++u) {
          u32x4 o;
#pragma unroll
          for (int j = 0; j < 4; ++j) o[j] = pk_bf16(gelu_tanh(acc[8 * u + 2 * j]), gelu_tanh(acc[8 * u + 2 * j + 1]));
          *(u32x4*)(dst + (size_t)u * 2 * 2048 * 16) = o;
        }
      } else {
        const bool do_gelu = (p < 6) && !is_q;
        int col;
        if (p >= 6) col = FH + single * 32;
        else if (is_q) col = FH + (cg - 12) * 384 + p * 64 + cs * 32;
        else col = (cg - 6) * 384 + p * 64 + cs * 32;
        col += lane & 31;
        const int tg0 = t0 / 8 + 4 * tgp + h;
        char* dst = ws + OFF_ZQ + ((size_t)tg0 * NZQ + col) * 16;
#pragma unroll
        for (int u = 0; u < 2; ++u) {
          u32x4 o;
          if (do_gelu) {
#pragma unroll
            for (int j = 0; j < 4; ++j) o[j] = pk_bf16(gelu_tanh(acc[8 * u + 2 * j]), gelu_tanh(acc[8 * u + 2 * j + 1]));
          } else {
#pragma unroll
            for (int j = 0; j < 4; ++j) o[j] = pk_bf16(acc[8 * u + 2 * j], acc[8 * u + 2 * j + 1]);
          }
          *(u32x4*)(dst + (size_t)u * 2 * NZQ * 16) = o;
        }
      }
    }
  }
}

__global__ __launch_bounds__(512, 2) void k0_inproj(const u16* __restrict__ x, const u16* __restrict__ wqkv,
                                                    const u16* __restrict__ w_in, char* __restrict__ ws) {
  __shared__ __attribute__((aligned(16))) char lds[6 * K0_SLOT];
  const int b = blockIdx.x, xcd = b & 7, idx = b >> 3;
  const int cg = xcd * 2 + (idx & 1), rt = idx >> 1;
  if (cg < 6) k0_body<1>(lds, x, wqkv, w_in, ws, rt, cg);
  else        k0_body<0>(lds, x, wqkv, w_in, ws, rt, cg);
}

constexpr int AT_TG = 320;
constexpr int AT_K = 0, AT_V = 64 * AT_TG, AT_Q = 2 * 64 * AT_TG, AT_ONES = AT_Q + 32 * AT_TG;
constexpr int K1_LDS = AT_ONES + 16;

DEVI void attn_role(char* lds, char* __restrict__ ws, int head, int qb) {
  const int tid = threadIdx.x, lane = tid & 63;
  const int w = __builtin_amdgcn_readfirstlane(tid >> 6);
  const int h = lane >> 5;
  const char* zq = ws + OFF_ZQ;
  const int tgk0 = 32 * qb - 32;
  for (int n = w; n < 50; n += 8) {
    int part, nn;
    if (n < 20) { part = 1; nn = n; } else if (n < 40) { part = 2; nn = n - 20; } else { part = 0; nn = n - 40; }
    int i = nn * 64 + lane;
    int tg = i / 20, d = i - tg * 20;
    d = d > 17 ? 17 : d;
    int gtg = (part == 0) ? 32 * qb + tg : tgk0 + tg;
    gtg = gtg < 0 ? 0 : gtg;
    const char* src = zq + ((size_t)gtg * NZQ + FH + 576 * part + 18 * head + d) * 16;
    char* dst = lds + (part == 1 ? AT_K : part == 2 ? AT_V : AT_Q) + nn * 1024;
    GLDS(src, dst);
  }
  if (tid == 0) { u32x4 o = {0x3f803f80u, 0x3f803f80u, 0x3f803f80u, 0x3f803f80u}; *(u32x4*)(lds + AT_ONES) = o; }
  WAIT_V(0);
  BAR();

  const int q4 = (lane & 15) >> 2, p4 = lane & 3, g16 = (lane >> 4) & 1;
  u32x4 qf0, qf1;
  {
    const char* qb_ = lds + AT_Q + (4 * w + 2 * g16 + (p4 >> 1)) * AT_TG + (p4 & 1) * 8;
    int d0 = 8 * h + q4;
    qf0 = tr_read2(qb_ + d0 * 16, qb_ + (d0 + 4) * 16);
    int d1 = 16 + q4; d1 = d1 > 17 ? 17 : d1;
    u32x2 t = tr_read(qb_ + d1 * 16);
    qf1[0] = h ? 0u : t[0]; qf1[1] = 0u; qf1[2] = 0u; qf1[3] = 0u;
  }
  const char* kb0 = lds + AT_K + (4 * w + 2 * g16 + (p4 & 1)) * AT_TG + (p4 >> 1) * 8;
  const int kd0 = (8 * h + q4) * 16;
  const int kd1 = (16 + q4 > 17 ? 17 : 16 + q4) * 16;
  const int n = lane & 31;
  const int jj0 = (qb == 0) ? (8 - w) : 0;
  const float NEG = -3.0e38f;

  auto scores = [&](int jj) -> f32x16 {
    const char* kb = kb0 + jj * 4 * AT_TG;
    u32x4 k0 = tr_read2(kb + kd0, kb + kd0 + 64);
    u32x2 k1a = tr_read(kb + kd1);
    u32x4 k1 = {k1a[0], k1a[1], k1a[0], k1a[1]};
    f32x16 s;
#pragma unroll
    for (int i = 0; i < 16; ++i) s[i] = 0.f;
    s = mfma32(k0, qf0, s);
    s = mfma32(k1, qf1, s);
    if (jj == 0) {
#pragma unroll
      for (int r = 0; r < 16; ++r) { int kap = (r & 7) + 8 * h + 16 * (r >> 3); s[r] = (kap > n) ? s[r] : NEG; }
    } else if (jj == 8) {
#pragma unroll
      for (int r = 0; r < 16; ++r) { int kap = (r & 7) + 8 * h + 16 * (r >> 3); s[r] = (kap <= n) ? s[r] : NEG; }
    }
    return s;
  };

  float m = NEG;
  for (int jj = jj0; jj <= 8; ++jj) {
    f32x16 s = scores(jj);
#pragma unroll
    for (int r = 0; r < 16; ++r) m = fmaxf(m, s[r]);
  }
  m = fmaxf(m, __shfl_xor(m, 32));
  const float c = 0.23570226039551584f * 1.4426950408889634f;
  const float mc = m * c;
  f32x16 o;
#pragma unroll
  for (int i = 0; i < 16; ++i) o[i] = 0.f;
  const int vd = lane & 31;
  const char* vb0 = (vd == 18) ? lds + AT_ONES : lds + AT_V + (4 * w + h) * AT_TG + vd * 16;
  const int vstep = (vd == 18) ? 0 : AT_TG;
  for (int jj = jj0; jj <= 8; ++jj) {
    f32x16 s = scores(jj);
    u32x4 p0, p1;
#pragma unroll
    for (int j = 0; j < 4; ++j) {
      p0[j] = pk_bf16(fexp2(__builtin_fmaf(s[2 * j], c, -mc)), fexp2(__builtin_fmaf(s[2 * j + 1], c, -mc)));
      p1[j] = pk_bf16(fexp2(__builtin_fmaf(s[8 + 2 * j], c, -mc)), fexp2(__builtin_fmaf(s[8 + 2 * j + 1], c, -mc)));
    }
    const char* vb = vb0 + jj * 4 * vstep;
    u32x4 v0 = *(const u32x4*)vb;
    u32x4 v1 = *(const u32x4*)(vb + 2 * vstep);
    o = mfma32(v0, p0, o);
    o = mfma32(v1, p1, o);
  }
  float l = o[10];
  float lo = __shfl_xor(l, 32);
  l = h ? lo : l;
  const float inv = 1.0f / l;
  const int t = 256 * qb + 32 * w + n;
  char* po = ws + OFF_PO + (size_t)t * 16;
  const int cb = 18 * head + 4 * h;
#pragma unroll
  for (int pr = 0; pr < 5; ++pr) {
    const int r = 2 * pr;
    const int d = (r & 3) + 8 * (r >> 2);
    if (pr < 4 || h == 0) {
      int cc = cb + d;
      u32 v = pk_bf16(o[r] * inv, o[r + 1] * inv);
      *(u32*)(po + (size_t)(288 + (cc >> 3)) * 2048 * 16 + (cc & 7) * 2) = v;
    }
  }
}

DEVI void z2n_role(char* lds, char* __restrict__ ws, int blk) {
  const int tid = threadIdx.x;
  const int half = tid >> 8, j = tid & 255, wv = tid >> 6;
  const int tg = blk * 2 + half;
  const char* src = ws + OFF_ZQ + ((size_t)tg * NZQ + j) * 16;
  u32x4 v[9];
#pragma unroll
  for (int i = 0; i < 9; ++i) v[i] = *(const u32x4*)(src + i * 4096);
  float* red = (float*)lds;
  float s[8];
#pragma unroll
  for (int e = 0; e < 8; ++e) s[e] = 0.f;
#pragma unroll
  for (int i = 0; i < 9; ++i) {
#pragma unroll
    for (int e = 0; e < 4; ++e) { s[2 * e] += bf_lo(v[i][e]); s[2 * e + 1] += bf_hi(v[i][e]); }
  }
#pragma unroll
  for (int e = 0; e < 8; ++e) {
#pragma unroll
    for (int o = 32; o >= 1; o >>= 1) s[e] += __shfl_xor(s[e], o);
  }
  if ((tid & 63) == 0) {
#pragma unroll
    for (int e = 0; e < 8; ++e) red[wv * 8 + e] = s[e];
  }
  __syncthreads();
  float mean[8];
#pragma unroll
  for (int e = 0; e < 8; ++e) {
    const float* r = red + half * 32 + e;
    mean[e] = (r[0] + r[8] + r[16] + r[24]) * (1.0f / 2304.0f);
    s[e] = 0.f;
  }
#pragma unroll
  for (int i = 0; i < 9; ++i) {
#pragma unroll
    for (int e = 0; e < 4; ++e) {
      float a = bf_lo(v[i][e]) - mean[2 * e], b = bf_hi(v[i][e]) - mean[2 * e + 1];
      s[2 * e] = __builtin_fmaf(a, a, s[2 * e]);
      s[2 * e + 1] = __builtin_fmaf(b, b, s[2 * e + 1]);
    }
  }
#pragma unroll
  for (int e = 0; e < 8; ++e) {
#pragma unroll
    for (int o = 32; o >= 1; o >>= 1) s[e] += __shfl_xor(s[e], o);
  }
  if ((tid & 63) == 0) {
#pragma unroll
    for (int e = 0; e < 8; ++e) red[64 + wv * 8 + e] = s[e];
  }
  __syncthreads();
  float rs[8], nb[8];
#pragma unroll
  for (int e = 0; e < 8; ++e) {
    const float* r = red + 64 + half * 32 + e;
    float var = (r[0] + r[8] + r[16] + r[24]) * (1.0f / 2304.0f);
    rs[e] = 1.0f / sqrtf(var + 1e-5f);
    nb[e] = -mean[e] * rs[e];
  }
  char* dst = ws + OFF_ZN + ((size_t)tg * FH + j) * 16;
#pragma unroll
  for (int i = 0; i < 9; ++i) {
    u32x4 o;
#pragma unroll
    for (int e = 0; e < 4; ++e)
      o[e] = pk_bf16(__builtin_fmaf(bf_lo(v[i][e]), rs[2 * e], nb[2 * e]),
                     __builtin_fmaf(bf_hi(v[i][e]), rs[2 * e + 1], nb[2 * e + 1]));
    *(u32x4*)(dst + i * 4096) = o;
  }
}

DEVI void tile_role(const u16* __restrict__ w_out, const u16* __restrict__ wo, char* __restrict__ ws, int blk) {
  const int g = blk * 512 + threadIdx.x;
  if (g >= 360 * 72) return;
  const int fc = g / 72, dq = g - fc * 72;
  const char* src = (fc < 288) ? (const char*)w_out + (size_t)fc * 8 * 1152 : (const char*)wo + (size_t)(fc - 288) * 8 * 1152;
  src += dq * 16;
  u32x4 r[8];
#pragma unroll
  for (int i = 0; i < 8; ++i) r[i] = *(const u32x4*)(src + i * 1152);
  char* dst = ws + OFF_WT + ((size_t)fc * 576 + dq * 8) * 16;
#pragma unroll
  for (int d = 0; d < 8; ++d) {
    u32x4 o;
#pragma unroll
    for (int m = 0; m < 4; ++m)
      o[m] = __builtin_amdgcn_perm(r[2 * m + 1][d >> 1], r[2 * m][d >> 1], (d & 1) ? 0x07060302u : 0x05040100u);
    *(u32x4*)(dst + d * 16) = o;
  }
}

__global__ __launch_bounds__(512, 4) void k1_mid(const u16* __restrict__ w_out, const u16* __restrict__ wo, char* __restrict__ ws) {
  __shared__ __attribute__((aligned(16))) char lds[K1_LDS];
  const int b = blockIdx.x;
  if (b < 256) {
    const int xcd = b & 7, idx = b >> 3;
    attn_role(lds, ws, xcd * 4 + (idx & 3), idx >> 2);
  } else if (b < 384) {
    z2n_role(lds, ws, b - 256);
  } else {
    tile_role(w_out, wo, ws, b - 384);
  }
}

constexpr int K2_SLOT = 16384 + 8 * 2304;

__global__ __launch_bounds__(512, 2) void k2_spatial(const u16* __restrict__ wsp, const u16* __restrict__ bsp, char* __restrict__ ws) {
  __shared__ __attribute__((aligned(16))) char lds[4 * K2_SLOT];
  const int tid = threadIdx.x, lane = tid & 63;
  const int w = __builtin_amdgcn_readfirstlane(tid >> 6);
  const int kh = w >> 2, tq = w & 3;
  const int b = blockIdx.x, xcd = b & 7, idx = b >> 3;
  const int tb = (xcd & 3) * 4 + (idx & 3), fb = (xcd >> 2) * 8 + (idx >> 2);
  const int t0 = tb * 128, f0 = fb * 144;

  const char* wsrc0;
  const char* wsrc1;
  {
    int r0 = 16 * w + (lane >> 3), r1 = r0 + 8;
    wsrc0 = (const char*)wsp + (size_t)(t0 + r0) * 4096 + (((lane & 7) ^ ((r0 >> 1) & 7)) * 16);
    wsrc1 = (const char*)wsp + (size_t)(t0 + r1) * 4096 + (((lane & 7) ^ ((r1 >> 1) & 7)) * 16);
  }
  const char* zsrc = ws + OFF_ZN + ((size_t)w * FH + f0 + lane) * 16;
  auto issue = [&](int it) {
    char* sl = lds + (it & 3) * K2_SLOT;
    GLDS(wsrc0 + it * 128, sl + w * 2048);
    GLDS(wsrc1 + it * 128, sl + w * 2048 + 1024);
    const char* z = zsrc + (size_t)it * 8 * FH * 16;
    char* zd = sl + 16384 + w * 2304;
    GLDS(z, zd);
    GLDS(z + 1024, zd + 1024);
    if (lane < 16) GLDS(z + 2048, zd + 2048);
  };
  issue(0); issue(1); issue(2);

  f32x4 acc[9][2];
#pragma unroll
  for (int i = 0; i < 9; ++i) { acc[i][0] = f32x4{0.f, 0.f, 0.f, 0.f}; acc[i][1] = f32x4{0.f, 0.f, 0.f, 0.f}; }
  const int g = lane >> 4, l15 = lane & 15;
  int woff[2];
#pragma unroll
  for (int ti = 0; ti < 2; ++ti) {
    int row = 32 * tq + 16 * ti + l15;
    woff[ti] = row * 128 + (((4 * kh + g) ^ ((row >> 1) & 7)) * 16);
  }
  const int zoff = 16384 + (4 * kh + g) * 2304 + l15 * 16;

  auto compute = [&](int it) {
    const char* sl = lds + (it & 3) * K2_SLOT;
    u32x4 b0 = *(const u32x4*)(sl + woff[0]);
    u32x4 b1 = *(const u32x4*)(sl + woff[1]);
#pragma unroll
    for (int fi = 0; fi < 9; ++fi) {
      u32x4 a = *(const u32x4*)(sl + zoff + fi * 256);
      acc[fi][0] = mfma16(a, b0, acc[fi][0]);
      acc[fi][1] = mfma16(a, b1, acc[fi][1]);
    }
  };
  for (int it = 0; it < 30; ++it) {
    WAIT_V(10); BAR();
    if (it < 29) issue(it + 3);
    compute(it);
  }
  WAIT_V(5); BAR(); compute(30);
  WAIT_V(0); BAR(); compute(31);

  const size_t eoff = ((size_t)(f0 / 8 + (g >> 1)) * 2048 + t0 + 32 * tq + l15) * 16 + (g & 1) * 8;
  float bias[2];
#pragma unroll
  for (int ti = 0; ti < 2; ++ti) bias[ti] = bf_lo((u32)bsp[t0 + 32 * tq + 16 * ti + l15]);
  BAR();
  char* xch = lds + tq * 18432 + lane * 16;
  auto finish = [&](auto KH) {
    constexpr int khc = decltype(KH)::value;
    constexpr int lo = khc ? 5 : 0, hi = khc ? 9 : 5;
    u32x2 z1v[5][2];
#pragma unroll
    for (int fi = lo; fi < hi; ++fi)
#pragma unroll
      for (int ti = 0; ti < 2; ++ti)
        z1v[fi - lo][ti] = *(const u32x2*)(ws + OFF_Z1 + eoff + ((size_t)fi * 2 * 2048 + ti * 16) * 16);
#pragma unroll
    for (int fi = 0; fi < 9; ++fi)
      if (fi < lo || fi >= hi) {
        *(f32x4*)(xch + (fi * 2 + 0) * 1024) = acc[fi][0];
        *(f32x4*)(xch + (fi * 2 + 1) * 1024) = acc[fi][1];
      }
    BAR();
#pragma unroll
    for (int fi = lo; fi < hi; ++fi)
#pragma unroll
      for (int ti = 0; ti < 2; ++ti) {
        f32x4 o = *(const f32x4*)(xch + (fi * 2 + ti) * 1024);
        f32x4 gq = acc[fi][ti] + o;
        u32x2 z = z1v[fi - lo][ti];
        u32x2 r;
        r[0] = pk_bf16(bf_lo(z[0]) * (gq[0] + bias[ti]), bf_hi(z[0]) * (gq[1] + bias[ti]));
        r[1] = pk_bf16(bf_lo(z[1]) * (gq[2] + bias[ti]), bf_hi(z[1]) * (gq[3] + bias[ti]));
        *(u32x2*)(ws + OFF_PO + eoff + ((size_t)fi * 2 * 2048 + ti * 16) * 16) = r;
      }
  };
  if (kh == 0) finish(std::integral_constant<int, 0>{});
  else         finish(std::integral_constant<int, 1>{});
}

constexpr int K3_SLOT = 8192 + 8 * 1280;

__global__ __launch_bounds__(512, 2) void k3_out(const u16* __restrict__ x, char* __restrict__ ws, u16* __restrict__ out) {
  __shared__ __attribute__((aligned(16))) char lds[8 * K3_SLOT];
  const int tid = threadIdx.x, lane = tid & 63;
  const int w = __builtin_amdgcn_readfirstlane(tid >> 6);
  const int kh = w >> 2, th = (w >> 1) & 1, dh = (w & 1) ^ kh;
  const int b = blockIdx.x, xcd = b & 7, idx = b >> 3;
  const int tb = xcd * 4 + (idx & 3), db = idx >> 2;
  const int t0 = tb * 64, d0 = db * 72;

  const char* psrc = ws + OFF_PO + ((size_t)w * 2048 + t0 + lane) * 16;
  const char* wsrc = ws + OFF_WT + ((size_t)w * 576 + d0 + lane) * 16;
  auto issue = [&](int it) {
    char* sl = lds + (it & 7) * K3_SLOT;
    GLDS(psrc + (size_t)it * 8 * 2048 * 16, sl + w * 1024);
    const char* ww = wsrc + (size_t)it * 8 * 576 * 16;
    char* wd = sl + 8192 + w * 1280;
    GLDS(ww, wd);
    if (lane < 8) GLDS(ww + 1024, wd + 1024);
  };
#pragma unroll
  for (int i = 0; i < 7; ++i) issue(i);

  const int g = lane >> 4, l15 = lane & 15;
  const int poff = (4 * kh + g) * 1024 + (32 * th + l15) * 16;
  const int aoff = 8192 + (4 * kh + g) * 1280 + (48 * dh + l15) * 16;
  f32x4 accB[3][2], accS[3][2];
#pragma unroll
  for (int i = 0; i < 3; ++i)
#pragma unroll
    for (int j = 0; j < 2; ++j) { accB[i][j] = f32x4{0.f, 0.f, 0.f, 0.f}; accS[i][j] = f32x4{0.f, 0.f, 0.f, 0.f}; }

  auto compute = [&](int it, f32x4 (&acc)[3][2]) {
    const char* sl = lds + (it & 7) * K3_SLOT;
    u32x4 b0 = *(const u32x4*)(sl + poff);
    u32x4 b1 = *(const u32x4*)(sl + poff + 256);
    u32x4 a0 = *(const u32x4*)(sl + aoff);
    u32x4 a1 = *(const u32x4*)(sl + aoff + 256);
    acc[0][0] = mfma16(a0, b0, acc[0][0]);
    acc[0][1] = mfma16(a0, b1, acc[0][1]);
    acc[1][0] = mfma16(a1, b0, acc[1][0]);
    acc[1][1] = mfma16(a1, b1, acc[1][1]);
    if (dh == 0) {
      u32x4 a2 = *(const u32x4*)(sl + aoff + 512);
      acc[2][0] = mfma16(a2, b0, acc[2][0]);
      acc[2][1] = mfma16(a2, b1, acc[2][1]);
    }
  };
  for (int it = 0; it < 36; ++it) {
    WAIT_V(18); BAR();
    issue(it + 7);
    compute(it, accB);
  }
  WAIT_V(18); BAR(); issue(43); compute(36, accS);
  WAIT_V(18); BAR(); issue(44); compute(37, accS);
  WAIT_V(18); BAR(); compute(38, accS);
  WAIT_V(15); BAR(); compute(39, accS);
  WAIT_V(12); BAR(); compute(40, accS);
  WAIT_V(9);  BAR(); compute(41, accS);
  WAIT_V(6);  BAR(); compute(42, accS);
  WAIT_V(3);  BAR(); compute(43, accS);
  WAIT_V(0);  BAR(); compute(44, accS);
  BAR();
  char* xch = lds + ((th * 2 + dh) * 2) * 6144 + lane * 16;
  auto finish = [&](auto KH) {
    constexpr int own = decltype(KH)::value, oth = own ^ 1;
#pragma unroll
    for (int di = 0; di < 3; ++di) {
      *(f32x4*)(xch + oth * 6144 + di * 1024) = accB[di][oth];
      *(f32x4*)(xch + oth * 6144 + 3072 + di * 1024) = accS[di][oth];
    }
    const int t = t0 + 32 * th + 16 * own + l15;
    u32x2 xv[3];
#pragma unroll
    for (int di = 0; di < 3; ++di) {
      int d = 48 * dh + 16 * di + 4 * g;
      d = d < 72 ? d : 0;
      xv[di] = *(const u32x2*)(x + (size_t)t * 576 + d0 + d);
    }
    BAR();
#pragma unroll
    for (int di = 0; di < 3; ++di) {
      f32x4 Bv = accB[di][own] + *(const f32x4*)(xch + own * 6144 + di * 1024);
      f32x4 Sv = accS[di][own] + *(const f32x4*)(xch + own * 6144 + 3072 + di * 1024);
      int d = 48 * dh + 16 * di + 4 * g;
      if (d < 72 && (di < 2 || dh == 0)) {
        u32x2 r;
        r[0] = pk_bf16(bf_lo(xv[di][0]) + Sv[0] * silu_f(Bv[0]), bf_hi(xv[di][0]) + Sv[1] * silu_f(Bv[1]));
        r[1] = pk_bf16(bf_lo(xv[di][1]) + Sv[2] * silu_f(Bv[2]), bf_hi(xv[di][1]) + Sv[3] * silu_f(Bv[3]));
        *(u32x2*)(out + (size_t)t * 576 + d0 + d) = r;
      }
    }
  };
  if (kh == 0) finish(std::integral_constant<int, 0>{});
  else         finish(std::integral_constant<int, 1>{});
}

static inline void launch_stage(int st, void* const* d_in, void* d_out, void* d_ws, hipStream_t stream) {
  const u16* x = (const u16*)d_in[0];
  const u16* wqkv = (const u16*)d_in[1];
  const u16* wo = (const u16*)d_in[2];
  const u16* w_in = (const u16*)d_in[4];
  const u16* w_sp = (const u16*)d_in[5];
  const u16* b_sp = (const u16*)d_in[6];
  const u16* w_out = (const u16*)d_in[7];
  char* ws = (char*)d_ws;
  if (st == 0) k0_inproj<<<256, 512, 0, stream>>>(x, wqkv, w_in, ws);
  if (st == 1) k1_mid<<<384 + 51, 512, 0, stream>>>(w_out, wo, ws);
  if (st == 2) k2_spatial<<<256, 512, 0, stream>>>(w_sp, b_sp, ws);
  if (st == 3) k3_out<<<256, 512, 0, stream>>>(x, ws, (u16*)d_out);
}

extern "C" void kernel_launch(void* const* d_in, const int* in_sizes, int n_in, void* d_out, int out_size, void* d_ws,
                              size_t ws_size, hipStream_t stream) {
  (void)in_sizes; (void)n_in; (void)out_size; (void)ws_size;
  for (int st = 0; st < 4; ++st) launch_stage(st, d_in, d_out, d_ws, stream);
}
```

```cpp
#include <hip/hip_runtime.h>
#include <hip/hip_bf16.h>
#include <stdint.h>
#include <type_traits>

typedef unsigned int u32;
typedef unsigned short u16;
typedef __attribute__((ext_vector_type(8))) __bf16 bf16x8;
typedef __attribute__((ext_vector_type(2))) __bf16 bf16x2;
typedef __attribute__((ext_vector_type(4))) short s16x4;
typedef __attribute__((ext_vector_type(2))) float f32x2;
typedef __attribute__((ext_vector_type(4))) float f32x4;
typedef __attribute__((ext_vector_type(16))) float f32x16;
typedef __attribute__((ext_vector_type(2))) u32 u32x2;
typedef __attribute__((ext_vector_type(4))) u32 u32x4;

#define DEVI __device__ __forceinline__
#define AS1 __attribute__((address_space(1)))
#define AS3 __attribute__((address_space(3)))
#define GLDS(g, l) __builtin_amdgcn_global_load_lds((const AS1 void*)(g), (AS3 void*)(l), 16, 0, 0)
#define WAIT_V(n) asm volatile("s_waitcnt vmcnt(" #n ")" ::: "memory")
#define WAIT_V_(n) asm volatile("s_waitcnt vmcnt(%0)" ::"n"(n) : "memory")
#define SB() __builtin_amdgcn_sched_barrier(0)
typedef __amdgpu_buffer_rsrc_t srd_t;
#define BUFL(m0v, voff, srd, soff) \
  asm volatile("s_mov_b32 m0, %0\n\ts_nop 0\n\tbuffer_load_dwordx4 %1, %2, %3 offen lds" ::"s"(m0v), "v"(voff), "s"(srd), "s"(soff) : "m0", "memory")
#define MAKE_SRD(p) __builtin_amdgcn_make_buffer_rsrc((void*)(p), (short)0, 0x7fffffff, 0x00020000)
#define LDS_ADDR(p) ((u32)(uintptr_t)(AS3 char*)(p))
#define WAIT_L0() asm volatile("s_waitcnt lgkmcnt(0)" ::: "memory")
#define SGB(m, n) __builtin_amdgcn_sched_group_barrier(m, n, 0)
#define BAR() do { WAIT_L0(); __builtin_amdgcn_s_barrier(); asm volatile("" ::: "memory"); } while (0)

constexpr int S_ = 2048, D_ = 576, FH = 2304, NZQ = 4032;
constexpr size_t OFF_Z1 = 0;
constexpr size_t OFF_ZQ = OFF_Z1 + (size_t)288 * 2048 * 16;
constexpr size_t OFF_ZN = OFF_ZQ + (size_t)256 * NZQ * 16;
constexpr size_t OFF_PO = OFF_ZN + (size_t)256 * FH * 16;
constexpr size_t OFF_WT = OFF_PO + (size_t)360 * 2048 * 16;
constexpr size_t WS_END = OFF_WT + (size_t)360 * 576 * 16;

#define STAMP(ws, k, i)
DEVI float bf_lo(u32 v) { return __builtin_bit_cast(float, v << 16); }
DEVI float bf_hi(u32 v) { return __builtin_bit_cast(float, v & 0xffff0000u); }
DEVI u32 pk_bf16(float a, float b) {
  f32x2 v = {a, b};
  return __builtin_bit_cast(u32, __builtin_convertvector(v, bf16x2));
}
DEVI f32x4 mfma16(u32x4 a, u32x4 b, f32x4 c) {
  return __builtin_amdgcn_mfma_f32_16x16x32_bf16(__builtin_bit_cast(bf16x8, a), __builtin_bit_cast(bf16x8, b), c, 0, 0, 0);
}
DEVI f32x16 mfma32(u32x4 a, u32x4 b, f32x16 c) {
  return __builtin_amdgcn_mfma_f32_32x32x16_bf16(__builtin_bit_cast(bf16x8, a), __builtin_bit_cast(bf16x8, b), c, 0, 0, 0);
}
DEVI u32x2 tr_read(const char* p) {
  s16x4 v = __builtin_amdgcn_ds_read_tr16_b64_v4i16((AS3 s16x4*)p);
  return __builtin_bit_cast(u32x2, v);
}
DEVI u32x4 tr_read2(const char* p0, const char* p1) {
  u32x2 a = tr_read(p0), b = tr_read(p1);
  u32x4 r = {a[0], a[1], b[0], b[1]};
  return r;
}
DEVI float fexp2(float x) { return __builtin_amdgcn_exp2f(x); }
DEVI float frcp(float x) { return __builtin_amdgcn_rcpf(x); }
DEVI float gelu_tanh(float v) {
  const float c0 = -2.0f * 0.7978845608028654f * 1.4426950408889634f;
  const float c1 = c0 * 0.044715f;
  float a = __builtin_fmaf(v * v, c1, c0);
  float e = fexp2(v * a);
  return v * frcp(1.0f + e);
}
DEVI float silu_f(float v) { return v * frcp(1.0f + fexp2(v * -1.4426950408889634f)); }
DEVI int swap23(int i) { return (i & ~12) | ((i & 4) << 1) | ((i & 8) >> 1); }

constexpr int K0_SLOT = 24576;

template <int ZORI>
DEVI void k0_body(char* lds, const u16* __restrict__ x, const u16* __restrict__ wqkv, const u16* __restrict__ w_in,
                  char* __restrict__ ws, int rt, int cg) {
  const int tid = threadIdx.x, lane = tid & 63;
  const int w = __builtin_amdgcn_readfirstlane(tid >> 6);
  const int tgp = w & 3, cs = w >> 2;
  const int h = lane >> 5;
  const int t0 = rt * 128;
  const int tok = swap23(lane & 31);

  {
    const char* xb = (const char*)x + (size_t)t0 * 1152;
#pragma unroll
    for (int th = 0; th < 3; ++th) {
#pragma unroll
      for (int n = 0; n < 6; ++n) {
        int i = (w * 6 + n) * 64 + lane;
        int row = i / 24, cp = i - row * 24;
        int c = (cp & ~7) | ((cp & 7) ^ ((row >> 1) & 7));
        GLDS(xb + row * 1152 + th * 384 + c * 16, lds + th * 49152 + (w * 6 + n) * 1024);
      }
    }
  }
  const bool is_q = cg >= 12;
  const int npair = (cg >= 6 && cg < 12) ? 7 : 6;
  const int nst = npair * 3;
  const int wrow = 24 * w + (lane >> 3);
  const int wch = (lane & 7) ^ (((lane >> 4) & 1) << 2);
  const char* wsrc_main = is_q ? (const char*)wqkv + (size_t)wrow * 3456 + (cg - 12) * 768 + wch * 16
                               : (const char*)w_in + (size_t)wrow * 9216 + cg * 768 + wch * 16;
  const int wstride_main = is_q ? 3456 : 9216;
  const int single = 48 + (cg - 6);
  const char* wsrc_single = (const char*)wqkv + (size_t)wrow * 3456 + (single >> 1) * 128 + wch * 16;
  auto issue = [&](int q) {
    int p = q / 3, th = q - p * 3;
    const char* src;
    int stride;
    if (p < 6) { src = wsrc_main + p * 128; stride = wstride_main; }
    else       { src = wsrc_single; stride = 3456; }
    src += (size_t)(th * 192) * stride;
    char* dst = lds + (q % 6) * K0_SLOT + w * 3072;
    GLDS(src, dst);
    GLDS(src + 8 * stride, dst + 1024);
    GLDS(src + 16 * stride, dst + 2048);
  };

  u32x4 xa[36];
  {
    const int row = 32 * tgp + tok;
    const int f = (row >> 1) & 7;
    const char* rb = lds + row * 384;
#define RDX(TH)                                                                          \
    _Pragma("unroll") for (int s = 0; s < 12; ++s) {                                     \
      int c = 2 * s + h;                                                                 \
      int cp = (c & ~7) | ((c & 7) ^ f);                                                 \
      xa[TH * 12 + s] = *(const u32x4*)(rb + TH * 49152 + cp * 16);                      \
    }
    WAIT_V(12); BAR();
    RDX(0)
    WAIT_V(6); BAR();
    issue(0); issue(1);
    RDX(1)
    WAIT_V(6); BAR();
    issue(2); issue(3);
    RDX(2)
    BAR();
    issue(4);
#undef RDX
  }
  {
    float s1 = 0.f, s2 = 0.f;
    const bf16x2 ones = __builtin_bit_cast(bf16x2, 0x3f803f80u);
#pragma unroll
    for (int i = 0; i < 36; ++i) {
#pragma unroll
      for (int j = 0; j < 4; ++j) {
        const u32 xv = xa[i][j];
        bf16x2 v = __builtin_bit_cast(bf16x2, xv);
        s1 = __builtin_amdgcn_fdot2_f32_bf16(v, ones, s1, false);
        s2 = __builtin_amdgcn_fdot2_f32_bf16(v, v, s2, false);
      }
    }
    s1 += __shfl_xor(s1, 32);
    s2 += __shfl_xor(s2, 32);
    float mean = s1 * (1.0f / 576.0f);
    float var = fmaxf(s2 * (1.0f / 576.0f) - mean * mean, 0.f);
    float rstd = 1.0f / sqrtf(var + 1e-5f);
    float nb = -mean * rstd;
#pragma unroll
    for (int i = 0; i < 36; ++i) {
#pragma unroll
      for (int j = 0; j < 4; ++j) {
        u32 v = xa[i][j];
        xa[i][j] = pk_bf16(__builtin_fmaf(bf_lo(v), rstd, nb), __builtin_fmaf(bf_hi(v), rstd, nb));
      }
    }
  }

  const int q4 = (lane & 15) >> 2, p4 = lane & 3, g16 = (lane >> 4) & 1;
  const int pp = ZORI ? (((p4 & 1) << 1) | (p4 >> 1)) : p4;
  const int rd_off = (8 * h + q4) * 128 + (((4 * cs + 2 * g16 + (pp >> 1)) ^ ((q4 >> 1) << 2)) * 16) + (pp & 1) * 8;

  for (int p = 0; p < npair; ++p) {
    f32x16 acc;
#pragma unroll
    for (int i = 0; i < 16; ++i) acc[i] = 0.f;
    const bool active = (p < 6) || ((single & 1) == cs);
#pragma unroll
    for (int th = 0; th < 3; ++th) {
      const int q = p * 3 + th;
      const int newer = nst - 1 - q;
      if (newer >= 4) WAIT_V(12);
      else if (newer == 3) WAIT_V(9);
      else if (newer == 2) WAIT_V(6);
      else if (newer == 1) WAIT_V(3);
      else WAIT_V(0);
      BAR();
      if (q + 5 < nst) issue(q + 5);
      if (active) {
        const char* sb = lds + (q % 6) * K0_SLOT + rd_off;
#pragma unroll
        for (int s = 0; s < 12; ++s) {
          u32x4 wf = tr_read2(sb + s * 2048, sb + s * 2048 + 512);
          if (ZORI) acc = mfma32(wf, xa[th * 12 + s], acc);
          else      acc = mfma32(xa[th * 12 + s], wf, acc);
        }
      }
    }
    if (active) {
      if (ZORI) {
        const int fc0 = (cg * 384 + p * 64 + cs * 32) / 8 + h;
        char* dst = ws + OFF_Z1 + ((size_t)fc0 * 2048 + t0 + 32 * tgp + tok) * 16;
#pragma unroll
        for (int u = 0; u < 2; ++u) {
          u32x4 o;
#pragma unroll
          for (int j = 0; j < 4; ++j) o[j] = pk_bf16(gelu_tanh(acc[8 * u + 2 * j]), gelu_tanh(acc[8 * u + 2 * j + 1]));
          *(u32x4*)(dst + (size_t)u * 2 * 2048 * 16) = o;
        }
      } else {
        const bool do_gelu = (p < 6) && !is_q;
        int col;
        if (p >= 6) col = FH + single * 32;
        else if (is_q) col = FH + (cg - 12) * 384 + p * 64 + cs * 32;
        else col = (cg - 6) * 384 + p * 64 + cs * 32;
        col += lane & 31;
        const int tg0 = t0 / 8 + 4 * tgp + h;
        char* dst = ws + OFF_ZQ + ((size_t)tg0 * NZQ + col) * 16;
#pragma unroll
        for (int u = 0; u < 2; ++u) {
          u32x4 o;
          if (do_gelu) {
#pragma unroll
            for (int j = 0; j < 4; ++j) o[j] = pk_bf16(gelu_tanh(acc[8 * u + 2 * j]), gelu_tanh(acc[8 * u + 2 * j + 1]));
          } else {
#pragma unroll
            for (int j = 0; j < 4; ++j) o[j] = pk_bf16(acc[8 * u + 2 * j], acc[8 * u + 2 * j + 1]);
          }
          *(u32x4*)(dst + (size_t)u * 2 * NZQ * 16) = o;
        }
      }
    }
  }
}

__global__ __launch_bounds__(512, 2) void k0_inproj(const u16* __restrict__ x, const u16* __restrict__ wqkv,
                                                    const u16* __restrict__ w_in, char* __restrict__ ws) {
  __shared__ __attribute__((aligned(16))) char lds[6 * K0_SLOT];
  const int b = blockIdx.x, xcd = b & 7, idx = b >> 3;
  const int cg = xcd * 2 + (idx & 1), rt = idx >> 1;
  if (cg < 6) k0_body<1>(lds, x, wqkv, w_in, ws, rt, cg);
  else        k0_body<0>(lds, x, wqkv, w_in, ws, rt, cg);
}

constexpr int AT_TG = 320;
constexpr int AT_K = 0, AT_V = 64 * AT_TG, AT_Q = 2 * 64 * AT_TG, AT_ONES = AT_Q + 32 * AT_TG;
constexpr int K1_LDS = AT_ONES + 16;

DEVI void attn_role(char* lds, char* __restrict__ ws, int head, int qb) {
  const int tid = threadIdx.x, lane = tid & 63;
  const int w = __builtin_amdgcn_readfirstlane(tid >> 6);
  const int h = lane >> 5;
  const char* zq = ws + OFF_ZQ;
  const int tgk0 = 32 * qb - 32;
  for (int n = w; n < 50; n += 8) {
    int part, nn;
    if (n < 20) { part = 1; nn = n; } else if (n < 40) { part = 2; nn = n - 20; } else { part = 0; nn = n - 40; }
    int i = nn * 64 + lane;
    int tg = i / 20, d = i - tg * 20;
    d = d > 17 ? 17 : d;
    int gtg = (part == 0) ? 32 * qb + tg : tgk0 + tg;
    gtg = gtg < 0 ? 0 : gtg;
    const char* src = zq + ((size_t)gtg * NZQ + FH + 576 * part + 18 * head + d) * 16;
    char* dst = lds + (part == 1 ? AT_K : part == 2 ? AT_V : AT_Q) + nn * 1024;
    GLDS(src, dst);
  }
  if (tid == 0) { u32x4 o = {0x3f803f80u, 0x3f803f80u, 0x3f803f80u, 0x3f803f80u}; *(u32x4*)(lds + AT_ONES) = o; }
  WAIT_V(0);
  BAR();

  const int q4 = (lane & 15) >> 2, p4 = lane & 3, g16 = (lane >> 4) & 1;
  u32x4 qf0, qf1;
  {
    const char* qb_ = lds + AT_Q + (4 * w + 2 * g16 + (p4 >> 1)) * AT_TG + (p4 & 1) * 8;
    int d0 = 8 * h + q4;
    qf0 = tr_read2(qb_ + d0 * 16, qb_ + (d0 + 4) * 16);
    int d1 = 16 + q4; d1 = d1 > 17 ? 17 : d1;
    u32x2 t = tr_read(qb_ + d1 * 16);
    qf1[0] = h ? 0u : t[0]; qf1[1] = 0u; qf1[2] = 0u; qf1[3] = 0u;
  }
  const char* kb0 = lds + AT_K + (4 * w + 2 * g16 + (p4 & 1)) * AT_TG + (p4 >> 1) * 8;
  const int kd0 = (8 * h + q4) * 16;
  const int kd1 = (16 + q4 > 17 ? 17 : 16 + q4) * 16;
  const int n = lane & 31;
  const int jj0 = (qb == 0) ? (8 - w) : 0;
  const float NEG = -3.0e38f;

  auto scores = [&](int jj) -> f32x16 {
    const char* kb = kb0 + jj * 4 * AT_TG;
    u32x4 k0 = tr_read2(kb + kd0, kb + kd0 + 64);
    u32x2 k1a = tr_read(kb + kd1);
    u32x4 k1 = {k1a[0], k1a[1], k1a[0], k1a[1]};
    f32x16 s;
#pragma unroll
    for (int i = 0; i < 16; ++i) s[i] = 0.f;
    s = mfma32(k0, qf0, s);
    s = mfma32(k1, qf1, s);
    if (jj == 0) {
#pragma unroll
      for (int r = 0; r < 16; ++r) { int kap = (r & 7) + 8 * h + 16 * (r >> 3); s[r] = (kap > n) ? s[r] : NEG; }
    } else if (jj == 8) {
#pragma unroll
      for (int r = 0; r < 16; ++r) { int kap = (r & 7) + 8 * h + 16 * (r >> 3); s[r] = (kap <= n) ? s[r] : NEG; }
    }
    return s;
  };

  float m = NEG;
  for (int jj = jj0; jj <= 8; ++jj) {
    f32x16 s = scores(jj);
#pragma unroll
    for (int r = 0; r < 16; ++r) m = fmaxf(m, s[r]);
  }
  m = fmaxf(m, __shfl_xor(m, 32));
  const float c = 0.23570226039551584f * 1.4426950408889634f;
  const float mc = m * c;
  f32x16 o;
#pragma unroll
  for (int i = 0; i < 16; ++i) o[i] = 0.f;
  const int vd = lane & 31;
  const char* vb0 = (vd == 18) ? lds + AT_ONES : lds + AT_V + (4 * w + h) * AT_TG + vd * 16;
  const int vstep = (vd == 18) ? 0 : AT_TG;
  for (int jj = jj0; jj <= 8; ++jj) {
    f32x16 s = scores(jj);
    u32x4 p0, p1;
#pragma unroll
    for (int j = 0; j < 4; ++j) {
      p0[j] = pk_bf16(fexp2(__builtin_fmaf(s[2 * j], c, -mc)), fexp2(__builtin_fmaf(s[2 * j + 1], c, -mc)));
      p1[j] = pk_bf16(fexp2(__builtin_fmaf(s[8 + 2 * j], c, -mc)), fexp2(__builtin_fmaf(s[8 + 2 * j + 1], c, -mc)));
    }
    const char* vb = vb0 + jj * 4 * vstep;
    u32x4 v0 = *(const u32x4*)vb;
    u32x4 v1 = *(const u32x4*)(vb + 2 * vstep);
    o = mfma32(v0, p0, o);
    o = mfma32(v1, p1, o);
  }
  float l = o[10];
  float lo = __shfl_xor(l, 32);
  l = h ? lo : l;
  const float inv = 1.0f / l;
  const int t = 256 * qb + 32 * w + n;
  char* po = ws + OFF_PO + (size_t)t * 16;
  const int cb = 18 * head + 4 * h;
#pragma unroll
  for (int pr = 0; pr < 5; ++pr) {
    const int r = 2 * pr;
    const int d = (r & 3) + 8 * (r >> 2);
    if (pr < 4 || h == 0) {
      int cc = cb + d;
      u32 v = pk_bf16(o[r] * inv, o[r + 1] * inv);
      *(u32*)(po + (size_t)(288 + (cc >> 3)) * 2048 * 16 + (cc & 7) * 2) = v;
    }
  }
}

DEVI void z2n_role(char* lds, char* __restrict__ ws, int blk) {
  const int tid = threadIdx.x;
  const int half = tid >> 8, j = tid & 255, wv = tid >> 6;
  const int tg = blk * 2 + half;
  const char* src = ws + OFF_ZQ + ((size_t)tg * NZQ + j) * 16;
  u32x4 v[9];
#pragma unroll
  for (int i = 0; i < 9; ++i) v[i] = *(const u32x4*)(src + i * 4096);
  float* red = (float*)lds;
  float s[8];
#pragma unroll
  for (int e = 0; e < 8; ++e) s[e] = 0.f;
#pragma unroll
  for (int i = 0; i < 9; ++i) {
#pragma unroll
    for (int e = 0; e < 4; ++e) { s[2 * e] += bf_lo(v[i][e]); s[2 * e + 1] += bf_hi(v[i][e]); }
  }
#pragma unroll
  for (int e = 0; e < 8; ++e) {
#pragma unroll
    for (int o = 32; o >= 1; o >>= 1) s[e] += __shfl_xor(s[e], o);
  }
  if ((tid & 63) == 0) {
#pragma unroll
    for (int e = 0; e < 8; ++e) red[wv * 8 + e] = s[e];
  }
  __syncthreads();
  float mean[8];
#pragma unroll
  for (int e = 0; e < 8; ++e) {
    const float* r = red + half * 32 + e;
    mean[e] = (r[0] + r[8] + r[16] + r[24]) * (1.0f / 2304.0f);
    s[e] = 0.f;
  }
#pragma unroll
  for (int i = 0; i < 9; ++i) {
#pragma unroll
    for (int e = 0; e < 4; ++e) {
      float a = bf_lo(v[i][e]) - mean[2 * e], b = bf_hi(v[i][e]) - mean[2 * e + 1];
      s[2 * e] = __builtin_fmaf(a, a, s[2 * e]);
      s[2 * e + 1] = __builtin_fmaf(b, b, s[2 * e + 1]);
    }
  }
#pragma unroll
  for (int e = 0; e < 8; ++e) {
#pragma unroll
    for (int o = 32; o >= 1; o >>= 1) s[e] += __shfl_xor(s[e], o);
  }
  if ((tid & 63) == 0) {
#pragma unroll
    for (int e = 0; e < 8; ++e) red[64 + wv * 8 + e] = s[e];
  }
  __syncthreads();
  float rs[8], nb[8];
#pragma unroll
  for (int e = 0; e < 8; ++e) {
    const float* r = red + 64 + half * 32 + e;
    float var = (r[0] + r[8] + r[16] + r[24]) * (1.0f / 2304.0f);
    rs[e] = 1.0f / sqrtf(var + 1e-5f);
    nb[e] = -mean[e] * rs[e];
  }
  char* dst = ws + OFF_ZN + ((size_t)tg * FH + j) * 16;
#pragma unroll
  for (int i = 0; i < 9; ++i) {
    u32x4 o;
#pragma unroll
    for (int e = 0; e < 4; ++e)
      o[e] = pk_bf16(__builtin_fmaf(bf_lo(v[i][e]), rs[2 * e], nb[2 * e]),
                     __builtin_fmaf(bf_hi(v[i][e]), rs[2 * e + 1], nb[2 * e + 1]));
    *(u32x4*)(dst + i * 4096) = o;
  }
}

DEVI void tile_role(const u16* __restrict__ w_out, const u16* __restrict__ wo, char* __restrict__ ws, int blk) {
  const int g = blk * 512 + threadIdx.x;
  if (g >= 360 * 72) return;
  const int fc = g / 72, dq = g - fc * 72;
  const char* src = (fc < 288) ? (const char*)w_out + (size_t)fc * 8 * 1152 : (const char*)wo + (size_t)(fc - 288) * 8 * 1152;
  src += dq * 16;
  u32x4 r[8];
#pragma unroll
  for (int i = 0; i < 8; ++i) r[i] = *(const u32x4*)(src + i * 1152);
  char* dst = ws + OFF_WT + ((size_t)fc * 576 + dq * 8) * 16;
#pragma unroll
  for (int d = 0; d < 8; ++d) {
    u32x4 o;
#pragma unroll
    for (int m = 0; m < 4; ++m)
      o[m] = __builtin_amdgcn_perm(r[2 * m + 1][d >> 1], r[2 * m][d >> 1], (d & 1) ? 0x07060302u : 0x05040100u);
    *(u32x4*)(dst + d * 16) = o;
  }
}

__global__ __launch_bounds__(512, 4) void k1_mid(const u16* __restrict__ w_out, const u16* __restrict__ wo, char* __restrict__ ws) {
  __shared__ __attribute__((aligned(16))) char lds[K1_LDS];
  const int b = blockIdx.x;
  if (b < 256) {
    const int xcd = b & 7, idx = b >> 3;
    attn_role(lds, ws, xcd * 4 + (idx & 3), idx >> 2);
  } else if (b < 384) {
    z2n_role(lds, ws, b - 256);
  } else {
    tile_role(w_out, wo, ws, b - 384);
  }
}

constexpr int K2_SLOT = 16384 + 8 * 2304;

template <int EXTRA>
DEVI void k2_body(char* lds, const u16* __restrict__ wsp, const u16* __restrict__ bsp, char* __restrict__ ws) {
  const int tid = threadIdx.x, lane = tid & 63;
  const int w = __builtin_amdgcn_readfirstlane(tid >> 6);
  const int kh = w >> 2, tq = w & 3;
  const int b = blockIdx.x, xcd = b & 7, idx = b >> 3;
  const int tb = (xcd & 3) * 4 + (idx & 3), fb = (xcd >> 2) * 8 + (idx >> 2);
  const int t0 = tb * 128, f0 = fb * 144;
  STAMP(ws, 2, 0);

  u32 wv0, wv1, zv[3];
  {
    int r0 = 16 * w + (lane >> 3), r1 = r0 + 8;
    wv0 = (u32)(r0 * 4096 + (((lane & 7) ^ ((r0 >> 1) & 7)) * 16));
    wv1 = (u32)(r1 * 4096 + (((lane & 7) ^ ((r1 >> 1) & 7)) * 16));
#pragma unroll
    for (int j = 0; j < 3; ++j) {
      int e = (w + 8 * j) * 64 + lane;
      int tg = e / 144, f = e - tg * 144;
      zv[j] = (u32)((tg * FH + f) * 16);
    }
  }
  const srd_t wsrd = MAKE_SRD((const char*)wsp + (size_t)t0 * 4096);
  const srd_t zsrd = MAKE_SRD(ws + OFF_ZN + (size_t)f0 * 16);
  const u32 ldsb = LDS_ADDR(lds) + w * 1024;
  auto piece = [&](int it, int j) {
    const u32 sl = ldsb + (it & 3) * K2_SLOT;
    const u32 wo = it * 128, zo = it * (8 * FH * 16);
    if (j == 0) BUFL(sl + w * 1024, wv0, wsrd, wo);
    if (j == 1) BUFL(sl + 16384, zv[0], zsrd, zo);
    if (j == 2) BUFL(sl + w * 1024 + 1024, wv1, wsrd, wo);
    if (j == 3) BUFL(sl + 16384 + 8192, zv[1], zsrd, zo);
    if (j == 4 && EXTRA) BUFL(sl + 16384 + 16384, zv[2], zsrd, zo);
  };
  auto issue = [&](int it) { piece(it, 0); piece(it, 1); piece(it, 2); piece(it, 3); piece(it, 4); };
#define K2_WAIT(n) do { if (EXTRA) WAIT_V_(n * 5); else WAIT_V_(n * 4); } while (0)
#define K2_WAITZ(n) do { if (EXTRA) WAIT_V_(n * 5 + 12); else WAIT_V_(n * 4 + 12); } while (0)
  issue(0); issue(1); issue(2); issue(3);

  const int g = lane >> 4, l15 = lane & 15;
  f32x4 acc[9][2];
#pragma unroll
  for (int i = 0; i < 9; ++i) { acc[i][0] = f32x4{0.f, 0.f, 0.f, 0.f}; acc[i][1] = f32x4{0.f, 0.f, 0.f, 0.f}; }
  int woff[2];
#pragma unroll
  for (int ti = 0; ti < 2; ++ti) {
    int row = 32 * tq + 16 * ti + l15;
    woff[ti] = row * 128 + (((4 * kh + g) ^ ((row >> 1) & 7)) * 16);
  }
  const int zoff = 16384 + (4 * kh + g) * 2304 + l15 * 16;

  struct Frags { u32x4 a[9]; u32x4 b[2]; };
  auto rdb = [&](int it, Frags& F) {
    const char* sl = lds + (it & 3) * K2_SLOT;
    F.b[0] = *(const u32x4*)(sl + woff[0]);
    F.b[1] = *(const u32x4*)(sl + woff[1]);
  };
  auto rda = [&](int it, Frags& F, int fi) {
    const char* sl = lds + (it & 3) * K2_SLOT;
    F.a[fi] = *(const u32x4*)(sl + zoff + fi * 256);
  };
  auto mm1 = [&](const Frags& F, int fi) {
    acc[fi][0] = mfma16(F.a[fi], F.b[0], acc[fi][0]);
    acc[fi][1] = mfma16(F.a[fi], F.b[1], acc[fi][1]);
  };
  auto stage = [&](const Frags& C, Frags& N, int rit, int lit, auto LDc, auto RDc) {
    constexpr bool LD = decltype(LDc)::value, RD = decltype(RDc)::value;
    SB();
    if (RD) rdb(rit, N);
    mm1(C, 0); SB();
    if (RD) { rda(rit, N, 0); rda(rit, N, 1); }
    mm1(C, 1); if (LD) piece(lit, 0); SB();
    if (RD) { rda(rit, N, 2); rda(rit, N, 3); }
    mm1(C, 2); SB();
    if (RD) { rda(rit, N, 4); rda(rit, N, 5); }
    mm1(C, 3); if (LD) piece(lit, 1); SB();
    if (RD) { rda(rit, N, 6); rda(rit, N, 7); }
    mm1(C, 4); SB();
    if (RD) rda(rit, N, 8);
    mm1(C, 5); if (LD) piece(lit, 2); SB();
    mm1(C, 6); SB();
    mm1(C, 7); if (LD) piece(lit, 3); SB();
    mm1(C, 8); if (LD) piece(lit, 4); SB();
  };
  using LDT = std::true_type;
  using T = std::true_type; using Fa = std::false_type;
  Frags F0, F1;
  K2_WAIT(3); BAR();
  STAMP(ws, 2, 1);
  rdb(0, F0);
#pragma unroll
  for (int fi = 0; fi < 9; ++fi) rda(0, F0, fi);
  for (int it = 0; it < 28; it += 2) {
    K2_WAIT(2); BAR();
    stage(F0, F1, it + 1, it + 4, LDT{}, T{});
    K2_WAIT(2); BAR();
    stage(F1, F0, it + 2, it + 5, LDT{}, T{});
  }
  STAMP(ws, 2, 2);
  const size_t eoff = ((size_t)(f0 / 8 + (g >> 1)) * 2048 + t0 + 32 * tq + l15) * 16 + (g & 1) * 8;
  u32x2 z1v[5][2];
  u32 biasr[2];
  K2_WAIT(2); BAR();
#pragma unroll
  for (int fi = 0; fi < 5; ++fi) {
    const int fa = (kh == 0) ? fi : (fi < 4 ? 5 + fi : 8);
#pragma unroll
    for (int ti = 0; ti < 2; ++ti)
      z1v[fi][ti] = *(const u32x2*)(ws + OFF_Z1 + eoff + ((size_t)fa * 2 * 2048 + ti * 16) * 16);
  }
#pragma unroll
  for (int ti = 0; ti < 2; ++ti) biasr[ti] = (u32)bsp[t0 + 32 * tq + 16 * ti + l15];
  stage(F0, F1, 29, 0, Fa{}, T{});
  K2_WAITZ(1); BAR(); stage(F1, F0, 30, 0, Fa{}, T{});
  K2_WAITZ(0); BAR(); stage(F0, F1, 31, 0, Fa{}, T{});
  stage(F1, F0, 0, 0, Fa{}, Fa{});
  STAMP(ws, 2, 3);
  float bias[2] = {bf_lo(biasr[0]), bf_lo(biasr[1])};
  BAR();
  char* xch = lds + tq * 18432 + lane * 16;
  auto finish = [&](auto KH) {
    constexpr int khc = decltype(KH)::value;
    constexpr int lo = khc ? 5 : 0, hi = khc ? 9 : 5;
#pragma unroll
    for (int fi = 0; fi < 9; ++fi)
      if (fi < lo || fi >= hi) {
        *(f32x4*)(xch + (fi * 2 + 0) * 1024) = acc[fi][0];
        *(f32x4*)(xch + (fi * 2 + 1) * 1024) = acc[fi][1];
      }
    BAR();
#pragma unroll
    for (int fi = lo; fi < hi; ++fi)
#pragma unroll
      for (int ti = 0; ti < 2; ++ti) {
        f32x4 o = *(const f32x4*)(xch + (fi * 2 + ti) * 1024);
        f32x4 gq = acc[fi][ti] + o;
        u32x2 z = z1v[fi - lo][ti];
        u32x2 r;
        r[0] = pk_bf16(bf_lo(z[0]) * (gq[0] + bias[ti]), bf_hi(z[0]) * (gq[1] + bias[ti]));
        r[1] = pk_bf16(bf_lo(z[1]) * (gq[2] + bias[ti]), bf_hi(z[1]) * (gq[3] + bias[ti]));
        *(u32x2*)(ws + OFF_PO + eoff + ((size_t)fi * 2 * 2048 + ti * 16) * 16) = r;
      }
  };
  if (kh == 0) finish(std::integral_constant<int, 0>{});
  else         finish(std::integral_constant<int, 1>{});
  STAMP(ws, 2, 4);
}

__global__ __launch_bounds__(512, 2) void k2_spatial(const u16* __restrict__ wsp, const u16* __restrict__ bsp, char* __restrict__ ws) {
  __shared__ __attribute__((aligned(16))) char lds[4 * K2_SLOT];
  if (__builtin_amdgcn_readfirstlane(threadIdx.x >> 6) < 2) k2_body<1>(lds, wsp, bsp, ws);
  else                   k2_body<0>(lds, wsp, bsp, ws);
}

constexpr int K3_SLOT = 8192 + 9216;

template <int EXTRA>
DEVI void k3_body(char* lds, const u16* __restrict__ x, char* __restrict__ ws, u16* __restrict__ out) {
  const int tid = threadIdx.x, lane = tid & 63;
  const int w = __builtin_amdgcn_readfirstlane(tid >> 6);
  const int kh = w >> 2, th = (w >> 1) & 1, dh = (w & 1) ^ kh;
  const int b = blockIdx.x, xcd = b & 7, idx = b >> 3;
  const int tb = xcd * 4 + (idx & 3), db = idx >> 2;
  const int t0 = tb * 64, d0 = db * 72;
  STAMP(ws, 3, 0);

  u32 pv = (u32)((w * 2048 + lane) * 16), wv[2];
#pragma unroll
  for (int j = 0; j < 2; ++j) {
    int e = (w + 8 * j) * 64 + lane;
    int fc = e / 72, dp = e - fc * 72;
    int d = ((fc & 1) && dp < 64) ? (dp ^ 8) : dp;
    wv[j] = (u32)((fc * 576 + d) * 16);
  }
  const srd_t psrd = MAKE_SRD(ws + OFF_PO + (size_t)t0 * 16);
  const srd_t wsrd = MAKE_SRD(ws + OFF_WT + (size_t)d0 * 16);
  const u32 ldsb = LDS_ADDR(lds) + w * 1024;
  auto piece = [&](int it, int j) {
    const u32 sl = ldsb + (it & 7) * K3_SLOT;
    if (j == 0) BUFL(sl, pv, psrd, it * (8 * 2048 * 16));
    if (j == 1) BUFL(sl + 8192, wv[0], wsrd, it * (8 * 576 * 16));
    if (j == 2 && EXTRA) BUFL(sl + 8192 + 8192, wv[1], wsrd, it * (8 * 576 * 16));
  };
  auto issue = [&](int it) { piece(it, 0); piece(it, 1); piece(it, 2); };
#define K3_WAIT(n, z) do { if (EXTRA) WAIT_V_((n) * 3 + (z)); else WAIT_V_((n) * 2 + (z)); } while (0)
#pragma unroll
  for (int i = 0; i < 8; ++i) issue(i);

  const int g = lane >> 4, l15 = lane & 15;
  const int poff = (4 * kh + g) * 1024 + (32 * th + l15) * 16;
  int aoff[3];
#pragma unroll
  for (int di = 0; di < 3; ++di) {
    int d = 48 * dh + 16 * di + l15;
    if (d >= 72) d = 64 + (l15 & 7);
    int dp = ((g & 1) && d < 64) ? (d ^ 8) : d;
    aoff[di] = 8192 + ((4 * kh + g) * 72 + dp) * 16;
  }
  f32x4 accB[3][2], accS[3][2];
#pragma unroll
  for (int i = 0; i < 3; ++i)
#pragma unroll
    for (int j = 0; j < 2; ++j) { accB[i][j] = f32x4{0.f, 0.f, 0.f, 0.f}; accS[i][j] = f32x4{0.f, 0.f, 0.f, 0.f}; }

  struct Frags { u32x4 a[3]; u32x4 b[2]; };
  auto body = [&](auto DHc) {
    constexpr int DH = decltype(DHc)::value;
    constexpr int NA = DH ? 2 : 3;
    auto rdb = [&](int it, Frags& F) {
      const char* sl = lds + (it & 7) * K3_SLOT;
      F.b[0] = *(const u32x4*)(sl + poff);
      F.b[1] = *(const u32x4*)(sl + poff + 256);
    };
    auto rda = [&](int it, Frags& F) {
      const char* sl = lds + (it & 7) * K3_SLOT;
#pragma unroll
      for (int di = 0; di < NA; ++di) F.a[di] = *(const u32x4*)(sl + aoff[di]);
    };
    auto mm1 = [&](const Frags& F, f32x4 (&acc)[3][2], int di) {
      acc[di][0] = mfma16(F.a[di], F.b[0], acc[di][0]);
      acc[di][1] = mfma16(F.a[di], F.b[1], acc[di][1]);
    };
    auto stage = [&](const Frags& C, Frags& N, f32x4 (&acc)[3][2], int rit, int lit, auto LDc, auto RDc) {
      constexpr bool LD = decltype(LDc)::value, RD = decltype(RDc)::value;
      SB();
      if (RD) rdb(rit, N);
      mm1(C, acc, 0); if (LD) piece(lit, 0); SB();
      if (RD) rda(rit, N);
      mm1(C, acc, 1); if (LD) piece(lit, 1); SB();
      if (NA == 3) mm1(C, acc, 2);
      if (LD) piece(lit, 2);
      SB();
    };
    using LDT = std::true_type;
    using T = std::true_type; using Fa = std::false_type;
    Frags F0, F1;
    K3_WAIT(7, 0); BAR();
    STAMP(ws, 3, 1);
    rdb(0, F0); rda(0, F0);
    for (int it = 0; it < 36; it += 2) {
      K3_WAIT(6, 0); BAR();
      stage(F0, F1, accB, it + 1, it + 8, LDT{}, T{});
      K3_WAIT(6, 0); BAR();
      stage(F1, F0, accB, it + 2, it + 9, LDT{}, T{});
    }
    STAMP(ws, 3, 2);
    K3_WAIT(6, 0); BAR();
    stage(F0, F1, accS, 37, 44, LDT{}, T{});
    const int t = t0 + 32 * th + 16 * kh + l15;
    u32x2 xv[3];
#pragma unroll
    for (int di = 0; di < 3; ++di) {
      int d = 48 * dh + 16 * di + 4 * g;
      d = d < 72 ? d : 0;
      xv[di] = *(const u32x2*)(x + (size_t)t * 576 + d0 + d);
    }
    K3_WAIT(6, 3); BAR(); stage(F1, F0, accS, 38, 0, Fa{}, T{});
    K3_WAIT(5, 3); BAR(); stage(F0, F1, accS, 39, 0, Fa{}, T{});
    K3_WAIT(4, 3); BAR(); stage(F1, F0, accS, 40, 0, Fa{}, T{});
    K3_WAIT(3, 3); BAR(); stage(F0, F1, accS, 41, 0, Fa{}, T{});
    K3_WAIT(2, 3); BAR(); stage(F1, F0, accS, 42, 0, Fa{}, T{});
    K3_WAIT(1, 3); BAR(); stage(F0, F1, accS, 43, 0, Fa{}, T{});
    K3_WAIT(0, 3); BAR(); stage(F1, F0, accS, 44, 0, Fa{}, T{});
    stage(F0, F1, accS, 0, 0, Fa{}, Fa{});
    STAMP(ws, 3, 3);
    BAR();
    char* xch = lds + ((th * 2 + dh) * 2) * 6144 + lane * 16;
    auto finish = [&](auto KH) {
      constexpr int own = decltype(KH)::value, oth = own ^ 1;
#pragma unroll
      for (int di = 0; di < NA; ++di) {
        *(f32x4*)(xch + oth * 6144 + di * 1024) = accB[di][oth];
        *(f32x4*)(xch + oth * 6144 + 3072 + di * 1024) = accS[di][oth];
      }
      BAR();
#pragma unroll
      for (int di = 0; di < NA; ++di) {
        f32x4 Bv = accB[di][own] + *(const f32x4*)(xch + own * 6144 + di * 1024);
        f32x4 Sv = accS[di][own] + *(const f32x4*)(xch + own * 6144 + 3072 + di * 1024);
        int d = 48 * dh + 16 * di + 4 * g;
        if (d < 72) {
          u32x2 r;
          r[0] = pk_bf16(bf_lo(xv[di][0]) + Sv[0] * silu_f(Bv[0]), bf_hi(xv[di][0]) + Sv[1] * silu_f(Bv[1]));
          r[1] = pk_bf16(bf_lo(xv[di][1]) + Sv[2] * silu_f(Bv[2]), bf_hi(xv[di][1]) + Sv[3] * silu_f(Bv[3]));
          *(u32x2*)(out + (size_t)t * 576 + d0 + d) = r;
        }
      }
    };
    if (kh == 0) finish(std::integral_constant<int, 0>{});
    else         finish(std::integral_constant<int, 1>{});
    STAMP(ws, 3, 4);
  };
  if (dh == 0) body(std::integral_constant<int, 0>{});
  else         body(std::integral_constant<int, 1>{});
}

__global__ __launch_bounds__(512, 2) void k3_out(const u16* __restrict__ x, char* __restrict__ ws, u16* __restrict__ out) {
  __shared__ __attribute__((aligned(16))) char lds[8 * K3_SLOT];
  if (__builtin_amdgcn_readfirstlane(threadIdx.x >> 6) == 0) k3_body<1>(lds, x, ws, out);
  else                                                       k3_body<0>(lds, x, ws, out);
}

__global__ void k_empty(char* ws) { if (ws == nullptr) ws[0] = 1; }
static inline void launch_stage(int st, void* const* d_in, void* d_out, void* d_ws, hipStream_t stream) {
  const u16* x = (const u16*)d_in[0];
  const u16* wqkv = (const u16*)d_in[1];
  const u16* wo = (const u16*)d_in[2];
  const u16* w_in = (const u16*)d_in[4];
  const u16* w_sp = (const u16*)d_in[5];
  const u16* b_sp = (const u16*)d_in[6];
  const u16* w_out = (const u16*)d_in[7];
  char* ws = (char*)d_ws;
  if (st == 0) k0_inproj<<<256, 512, 0, stream>>>(x, wqkv, w_in, ws);
  if (st == 1) k1_mid<<<384 + 51, 512, 0, stream>>>(w_out, wo, ws);
  if (st == 2) k2_spatial<<<256, 512, 0, stream>>>(w_sp, b_sp, ws);
  if (st == 4) k_empty<<<256, 512, 0, stream>>>(ws);
  if (st == 3) k3_out<<<256, 512, 0, stream>>>(x, ws, (u16*)d_out);
}

extern "C" void kernel_launch(void* const* d_in, const int* in_sizes, int n_in, void* d_out, int out_size, void* d_ws,
                              size_t ws_size, hipStream_t stream) {
  (void)in_sizes; (void)n_in; (void)out_size; (void)ws_size;
  for (int st = 0; st < 4; ++st) launch_stage(st, d_in, d_out, d_ws, stream);
}
```

```cpp
#include <hip/hip_runtime.h>
#include <hip/hip_bf16.h>
#include <stdint.h>
#include <type_traits>

typedef unsigned int u32;
typedef unsigned short u16;
typedef __attribute__((ext_vector_type(8))) __bf16 bf16x8;
typedef __attribute__((ext_vector_type(2))) __bf16 bf16x2;
typedef __attribute__((ext_vector_type(4))) short s16x4;
typedef __attribute__((ext_vector_type(2))) float f32x2;
typedef __attribute__((ext_vector_type(4))) float f32x4;
typedef __attribute__((ext_vector_type(16))) float f32x16;
typedef __attribute__((ext_vector_type(2))) u32 u32x2;
typedef __attribute__((ext_vector_type(4))) u32 u32x4;

#define DEVI __device__ __forceinline__
#define AS1 __attribute__((address_space(1)))
#define AS3 __attribute__((address_space(3)))
#define GLDS(g, l) __builtin_amdgcn_global_load_lds((const AS1 void*)(g), (AS3 void*)(l), 16, 0, 0)
#define WAIT_V(n) asm volatile("s_waitcnt vmcnt(" #n ")" ::: "memory")
#define WAIT_V_(n) asm volatile("s_waitcnt vmcnt(%0)" ::"n"(n) : "memory")
#define SB() __builtin_amdgcn_sched_barrier(0)
typedef __amdgpu_buffer_rsrc_t srd_t;
#define BUFL(m0v, voff, srd, soff) \
  asm volatile("s_mov_b32 m0, %0\n\ts_nop 0\n\tbuffer_load_dwordx4 %1, %2, %3 offen lds" ::"s"(m0v), "v"(voff), "s"(srd), "s"(soff) : "m0", "memory")
#define MAKE_SRD(p) __builtin_amdgcn_make_buffer_rsrc((void*)(p), (short)0, 0x7fffffff, 0x00020000)
#define LDS_ADDR(p) ((u32)(uintptr_t)(AS3 char*)(p))
#define WAIT_L0() asm volatile("s_waitcnt lgkmcnt(0)" ::: "memory")
#define SGB(m, n) __builtin_amdgcn_sched_group_barrier(m, n, 0)
#define BAR() do { WAIT_L0(); __builtin_amdgcn_s_barrier(); asm volatile("" ::: "memory"); } while (0)

constexpr int S_ = 2048, D_ = 576, FH = 2304, NZQ = 4032;
constexpr size_t OFF_Z1 = 0;
constexpr size_t OFF_ZQ = OFF_Z1 + (size_t)288 * 2048 * 16;
constexpr size_t OFF_ZN = OFF_ZQ + (size_t)256 * NZQ * 16;
constexpr size_t OFF_PO = OFF_ZN + (size_t)256 * FH * 16;
constexpr size_t OFF_WT = OFF_PO + (size_t)360 * 2048 * 16;
constexpr size_t WS_END = OFF_WT + (size_t)360 * 576 * 16;

#define STAMP(ws, k, i)
DEVI float bf_lo(u32 v) { return __builtin_bit_cast(float, v << 16); }
DEVI float bf_hi(u32 v) { return __builtin_bit_cast(float, v & 0xffff0000u); }
DEVI u32 pk_bf16(float a, float b) {
  f32x2 v = {a, b};
  return __builtin_bit_cast(u32, __builtin_convertvector(v, bf16x2));
}
DEVI f32x4 mfma16(u32x4 a, u32x4 b, f32x4 c) {
  return __builtin_amdgcn_mfma_f32_16x16x32_bf16(__builtin_bit_cast(bf16x8, a), __builtin_bit_cast(bf16x8, b), c, 0, 0, 0);
}
DEVI f32x16 mfma32(u32x4 a, u32x4 b, f32x16 c) {
  return __builtin_amdgcn_mfma_f32_32x32x16_bf16(__builtin_bit_cast(bf16x8, a), __builtin_bit_cast(bf16x8, b), c, 0, 0, 0);
}
DEVI u32x2 tr_read(const char* p) {
  s16x4 v = __builtin_amdgcn_ds_read_tr16_b64_v4i16((AS3 s16x4*)p);
  return __builtin_bit_cast(u32x2, v);
}
DEVI u32x4 tr_read2(const char* p0, const char* p1) {
  u32x2 a = tr_read(p0), b = tr_read(p1);
  u32x4 r = {a[0], a[1], b[0], b[1]};
  return r;
}
DEVI float fexp2(float x) { return __builtin_amdgcn_exp2f(x); }
DEVI float frcp(float x) { return __builtin_amdgcn_rcpf(x); }
DEVI float gelu_tanh(float v) {
  const float c0 = -2.0f * 0.7978845608028654f * 1.4426950408889634f;
  const float c1 = c0 * 0.044715f;
  float a = __builtin_fmaf(v * v, c1, c0);
  float e = fexp2(v * a);
  return v * frcp(1.0f + e);
}
DEVI float silu_f(float v) { return v * frcp(1.0f + fexp2(v * -1.4426950408889634f)); }
DEVI int swap23(int i) { return (i & ~12) | ((i & 4) << 1) | ((i & 8) >> 1); }

constexpr int K0_SLOT = 24576;
#define MAKE_SRDN(p, n) __builtin_amdgcn_make_buffer_rsrc((void*)(p), (short)0, (int)(n), 0x00020000)
template <int V> using IC = std::integral_constant<int, V>;
DEVI void bufl(u32 m0v, u32 voff, srd_t srd, u32 soff) { BUFL(m0v, voff, srd, soff); }
DEVI float dpp_xor1(float v) { return __builtin_bit_cast(float, __builtin_amdgcn_update_dpp(0, __builtin_bit_cast(int, v), 0xB1, 0xf, 0xf, true)); }
DEVI float dpp_xor2(float v) { return __builtin_bit_cast(float, __builtin_amdgcn_update_dpp(0, __builtin_bit_cast(int, v), 0x4E, 0xf, 0xf, true)); }

template <int ZORI, int GELU, int NP7>
DEVI void k0_body(char* lds, const u16* __restrict__ x, const u16* __restrict__ wqkv, const u16* __restrict__ w_in,
                  const u16* __restrict__ w_sp, const u16* __restrict__ w_out, const u16* __restrict__ wo,
                  char* __restrict__ ws, int rt, int cg) {
  const int tid = threadIdx.x, lane = tid & 63;
  const int w = __builtin_amdgcn_readfirstlane(tid >> 6);
  const int tgp = w & 3, cs = w >> 2;
  const int h = lane >> 5;
  const int t0 = rt * 128;
  const int tok = swap23(lane & 31);
  constexpr int NPAIR = 6 + NP7;
  constexpr bool IS_Q = !GELU;
  STAMP(ws, 0, 0);

  u32x4 xr[18];
  const int xrow = 16 * w + (lane >> 2), xj = lane & 3;
  {
    const char* xp = (const char*)x + (size_t)(t0 + xrow) * 1152 + xj * 16;
#pragma unroll
    for (int i = 0; i < 18; ++i) xr[i] = *(const u32x4*)(xp + i * 64);
  }
  constexpr int WSTR = IS_Q ? 3456 : 9216;
  const int wrow = 24 * w + (lane >> 3);
  const int wch = (lane & 7) ^ (((lane >> 4) & 1) << 2);
  const srd_t srdW = MAKE_SRD(IS_Q ? (const char*)wqkv + (cg - 12) * 768 : (const char*)w_in + cg * 768);
  const u32 voffW = wrow * WSTR + wch * 16;
  const int single = 48 + (cg - 6);
  const srd_t srdS = MAKE_SRD((const char*)wqkv + (NP7 ? (single >> 1) * 128 : 0));
  const u32 voffS = wrow * 3456 + wch * 16;
  const u32 ldsw = LDS_ADDR(lds) + w * 3072;
  const srd_t srdP0 = MAKE_SRD((const char*)w_sp + (size_t)blockIdx.x * 32768);
  const srd_t srdZ = MAKE_SRDN(x, 0);
  const u32 pidx = blockIdx.x * 8 + w;
  const u32 vl16 = lane * 16;
  auto issue = [&](auto PQ_, auto TH_, auto SL_, auto N_) {
    constexpr int pq = PQ_.value, TH = TH_.value, SL = SL_.value, N = N_.value;
    const u32 m0v = ldsw + SL * K0_SLOT + N * 1024;
    if constexpr (pq < 6) {
      bufl(m0v, voffW, srdW, (u32)(pq * 128 + (TH * 192 + 8 * N) * WSTR));
    } else if constexpr (NP7 && pq == 6) {
      bufl(m0v, voffS, srdS, (u32)((TH * 192 + 8 * N) * 3456));
    } else {
      bufl(m0v, vl16, srdZ, 0u);
    }
  };
  auto pf = [&](auto I_) {
    constexpr int i = I_.value;
    const u32 m0v = LDS_ADDR(lds) + 6 * K0_SLOT + w * 1024;
    if constexpr (i < 4) {
      bufl(m0v, vl16, srdP0, (w * 4 + i) * 1024);
    } else if constexpr (i < 6) {
      const u32 c = pidx * 2 + (i - 4);
      const bool ok = c < 2592;
      bufl(m0v, vl16, MAKE_SRDN(w_out, ok ? FH * D_ * 2 : 0), ok ? c * 1024 : 0u);
    } else if constexpr (i == 6) {
      const bool ok = pidx < 648;
      bufl(m0v, vl16, MAKE_SRDN(wo, ok ? D_ * D_ * 2 : 0), ok ? pidx * 1024 : 0u);
    }
  };
  auto issue3 = [&](auto pq, auto TH_, auto SL_) { issue(pq, TH_, SL_, IC<0>{}); issue(pq, TH_, SL_, IC<1>{}); issue(pq, TH_, SL_, IC<2>{}); };

  u32x4 xa[36];
  {
    float s1 = 0.f, s2 = 0.f;
    const bf16x2 ones = __builtin_bit_cast(bf16x2, 0x3f803f80u);
#pragma unroll
    for (int i = 0; i < 18; ++i) {
#pragma unroll
      for (int j = 0; j < 4; ++j) {
        const u32 xv = xr[i][j];
        bf16x2 v = __builtin_bit_cast(bf16x2, xv);
        s1 = __builtin_amdgcn_fdot2_f32_bf16(v, ones, s1, false);
        s2 = __builtin_amdgcn_fdot2_f32_bf16(v, v, s2, false);
      }
    }
    s1 += dpp_xor1(s1); s2 += dpp_xor1(s2);
    s1 += dpp_xor2(s1); s2 += dpp_xor2(s2);
    const float mean = s1 * (1.0f / 576.0f);
    const float var = fmaxf(s2 * (1.0f / 576.0f) - mean * mean, 0.f);
    const float rstd = __builtin_amdgcn_rsqf(var + 1e-5f);
    const float nb = -mean * rstd;
    const f32x2 r2 = {rstd, rstd}, n2 = {nb, nb};
    STAMP(ws, 0, 1);
    auto norm = [&](int i) {
      f32x2 f[4];
#pragma unroll
      for (int j = 0; j < 4; ++j) { const u32 v = xr[i][j]; f[j][0] = bf_lo(v); f[j][1] = bf_hi(v); }
#pragma unroll
      for (int j = 0; j < 4; ++j) f[j] = __builtin_elementwise_fma(f[j], r2, n2);
      u32x4 o;
#pragma unroll
      for (int j = 0; j < 4; ++j) o[j] = pk_bf16(f[j][0], f[j][1]);
      return o;
    };
    const int fw = (xrow >> 1) & 7;
    char* wA = lds + xrow * 384 + ((xj ^ fw) * 16);
    char* wB = lds + xrow * 384 + (((4 + xj) ^ fw) * 16);
    SB();
    auto nw = [&](auto I_) {
      constexpr int I = I_.value, th = I / 6, m = I % 6;
      *(u32x4*)(((m & 1) ? wB : wA) + th * 49152 + (m >> 1) * 128) = norm(I);
      if constexpr (I & 1) issue(IC<0>{}, IC<(I / 6)>{}, IC<4 + I / 6>{}, IC<(I >> 1) % 3>{});
      SB();
    };
    nw(IC<0>{}); nw(IC<1>{}); nw(IC<2>{}); nw(IC<3>{}); nw(IC<4>{}); nw(IC<5>{});
    nw(IC<6>{}); nw(IC<7>{}); nw(IC<8>{}); nw(IC<9>{}); nw(IC<10>{}); nw(IC<11>{});
    const int row = 32 * tgp + tok;
    const int f = (row >> 1) & 7;
    const char* rb = lds + row * 384;
#define RDX(TH, BASE)                                                                    \
    _Pragma("unroll") for (int s = 0; s < 12; ++s) {                                     \
      int c = 2 * s + h;                                                                 \
      int cp = (c & ~7) | ((c & 7) ^ f);                                                 \
      xa[TH * 12 + s] = *(const u32x4*)(rb + BASE + cp * 16);                            \
    }
    STAMP(ws, 0, 2);
    BAR();
    RDX(0, 0)
    RDX(1, 49152)
    SB();
#pragma unroll
    for (int i = 12; i < 18; ++i) xr[i] = norm(i);
    SB();
    BAR();
    STAMP(ws, 0, 3);
#pragma unroll
    for (int i = 12; i < 18; ++i) {
      const int m = i - 12;
      *(u32x4*)(((m & 1) ? wB : wA) + 49152 + (m >> 1) * 128) = xr[i];
    }
    BAR();
    RDX(2, 49152)
    SB();
    issue3(IC<0>{}, IC<2>{}, IC<0>{});
    issue3(IC<1>{}, IC<0>{}, IC<1>{});
    SB();
    WAIT_L0();
#undef RDX
  }
  STAMP(ws, 0, 4);

  const int q4 = (lane & 15) >> 2, p4 = lane & 3, g16 = (lane >> 4) & 1;
  const int pp = ZORI ? (((p4 & 1) << 1) | (p4 >> 1)) : p4;
  const int rd_off = (8 * h + q4) * 128 + (((4 * cs + 2 * g16 + (pp >> 1)) ^ ((q4 >> 1) << 2)) * 16) + (pp & 1) * 8;
  u32 rdb0 = LDS_ADDR(lds) + rd_off, rdb1 = rdb0 + 2 * K0_SLOT, rdb2 = rdb0 + 4 * K0_SLOT;
  asm volatile("" : "+v"(rdb0), "+v"(rdb1), "+v"(rdb2));
  char* dst0;
  size_t ustep;
  constexpr size_t PSTEP = ZORI ? (size_t)8 * 2048 * 16 : (size_t)64 * 16;
  if (ZORI) {
    dst0 = ws + OFF_Z1 + ((size_t)((cg * 384 + cs * 32) / 8 + h) * 2048 + t0 + 32 * tgp + tok) * 16;
    ustep = (size_t)2 * 2048 * 16;
  } else {
    const int col = (IS_Q ? FH + (cg - 12) * 384 : (cg - 6) * 384) + cs * 32 + (lane & 31);
    dst0 = ws + OFF_ZQ + ((size_t)(t0 / 8 + 4 * tgp + h) * NZQ + col) * 16;
    ustep = (size_t)2 * NZQ * 16;
  }
  u32x4 wf[4];
  auto rdf = [&](int slot, int s) {
    const u32 a = (slot < 2 ? rdb0 : slot < 4 ? rdb1 : rdb2) + (slot & 1) * K0_SLOT + s * 2048;
    const u32x2 lo = __builtin_bit_cast(u32x2, __builtin_amdgcn_ds_read_tr16_b64_v4i16((AS3 s16x4*)(uintptr_t)a));
    const u32x2 hi = __builtin_bit_cast(u32x2, __builtin_amdgcn_ds_read_tr16_b64_v4i16((AS3 s16x4*)(uintptr_t)(a + 512)));
    const u32x4 r = {lo[0], lo[1], hi[0], hi[1]};
    return r;
  };
  f32x2 ee;
  u32x4 eo;
  auto piece = [&](auto K_, const f32x16& pv, char* dst) {
    constexpr int K = K_.value, J = K >> 1;
    const f32x2 v = {pv[2 * J], pv[2 * J + 1]};
    if constexpr ((K & 1) == 0) {
      if (GELU) {
        const float c0 = -2.0f * 0.7978845608028654f * 1.4426950408889634f;
        const float c1 = c0 * 0.044715f;
        const f32x2 c0v = {c0, c0}, c1v = {c1, c1};
        f32x2 a = __builtin_elementwise_fma(v * v, c1v, c0v);
        f32x2 m = v * a;
        ee[0] = fexp2(m[0]); ee[1] = fexp2(m[1]);
      }
    } else {
      if (GELU) {
        const f32x2 one = {1.f, 1.f};
        f32x2 d = ee + one;
        f32x2 r = {frcp(d[0]), frcp(d[1])};
        f32x2 o = v * r;
        eo[J & 3] = pk_bf16(o[0], o[1]);
      } else {
        eo[J & 3] = pk_bf16(v[0], v[1]);
      }
      if ((J & 3) == 3) *(u32x4*)(dst + (J >> 2) * ustep) = eo;
    }
  };
  auto pair = [&](auto P_, auto SPL_, f32x16& acc, const f32x16& pv) {
    constexpr int p = P_.value, ODD = p & 1, PREV = p > 0, SPL = SPL_.value;
    constexpr int NS = SPL ? 6 : 12, S0 = SPL ? 6 * (SPL - 1) : 0;
    char* dst = dst0 + (size_t)(p - 1) * PSTEP;
#pragma unroll
    for (int i = 0; i < 16; ++i) acc[i] = 0.f;
    auto stage = [&](auto TH_) {
      constexpr int TH = TH_.value;
      constexpr int SL = (3 * ODD + TH + 4) % 6, SN = (SL + 1) % 6, SI = (SL + 5) % 6;
      constexpr int ITH = (TH + 2) % 3;
      constexpr bool FIRST = !PREV && TH == 0;
      constexpr int ip = p + (TH == 0 ? 1 : 2);
      if (FIRST || (SPL && TH == 1)) WAIT_V(6); else if (!(SPL && TH == 2)) WAIT_V(9);
      __builtin_amdgcn_s_barrier();
      SB();
      if ((!PREV || SPL) && TH == 0) { wf[0] = rdf(SL, S0); wf[1] = rdf(SL, S0 + 1); wf[2] = rdf(SL, S0 + 2); SB(); }
      auto step = [&](auto J_) {
        constexpr int J = J_.value, S = S0 + J, G = TH * NS + J;
        if constexpr (J < NS) {
          if (ZORI) acc = mfma32(wf[G & 3], xa[TH * 12 + S], acc);
          else      acc = mfma32(xa[TH * 12 + S], wf[G & 3], acc);
          SB();
          if (!(SPL && TH == 2 && J + 3 >= NS)) wf[(G + 3) & 3] = (J + 3 < NS) ? rdf(SL, S + 3) : rdf(SN, S0 + J + 3 - NS);
          if constexpr (!SPL) {
            if constexpr (FIRST && (S == 0 || S == 2 || S == 4)) issue(IC<1>{}, IC<1>{}, IC<2>{}, IC<S / 2>{});
            if constexpr (S == (FIRST ? 6 : 1)) issue(IC<ip>{}, IC<ITH>{}, IC<SI>{}, IC<0>{});
            if constexpr (S == (FIRST ? 8 : 5)) issue(IC<ip>{}, IC<ITH>{}, IC<SI>{}, IC<1>{});
            if constexpr (S == (FIRST ? 10 : 9)) issue(IC<ip>{}, IC<ITH>{}, IC<SI>{}, IC<2>{});
            if constexpr (S == 11 && PREV) pf(IC<(p - 1) * 3 + TH>{});
          }
          if constexpr (PREV && !SPL && (G & 1) && G < 32) piece(IC<(G >> 1)>{}, pv, dst);
          if constexpr (PREV && SPL && G < 16) piece(IC<G>{}, pv, dst);
          SB();
        }
      };
      step(IC<0>{}); step(IC<1>{}); step(IC<2>{}); step(IC<3>{}); step(IC<4>{}); step(IC<5>{});
      step(IC<6>{}); step(IC<7>{}); step(IC<8>{}); step(IC<9>{}); step(IC<10>{}); step(IC<11>{});
    };
    stage(IC<0>{}); stage(IC<1>{}); stage(IC<2>{});
  };
  f32x16 accA, accB;
  pair(IC<0>{}, IC<0>{}, accA, accA);
  pair(IC<1>{}, IC<0>{}, accB, accA);
  pair(IC<2>{}, IC<0>{}, accA, accB);
  pair(IC<3>{}, IC<0>{}, accB, accA);
  pair(IC<4>{}, IC<0>{}, accA, accB);
  pair(IC<5>{}, IC<0>{}, accB, accA);
  STAMP(ws, 0, 5);
  if (NP7) {
    if ((single & 1) != cs) { rdb0 ^= 64; rdb1 ^= 64; rdb2 ^= 64; }
    char* xch = lds + 4 * K0_SLOT + lane * 16;
    char* dst = ws + OFF_ZQ + ((size_t)(t0 / 8 + 4 * tgp + h) * NZQ + FH + single * 32 + (lane & 31)) * 16;
    auto tail = [&](auto CS_) {
      constexpr int CS = CS_.value;
      pair(IC<6>{}, IC<1 + CS>{}, accA, accB);
      f32x4 s0, s1;
#pragma unroll
      for (int j = 0; j < 4; ++j) { s0[j] = accA[8 * (1 - CS) + j]; s1[j] = accA[8 * (1 - CS) + 4 + j]; }
      *(f32x4*)(xch + w * 2048) = s0;
      *(f32x4*)(xch + w * 2048 + 1024) = s1;
      BAR();
      s0 = *(const f32x4*)(xch + (w ^ 4) * 2048);
      s1 = *(const f32x4*)(xch + (w ^ 4) * 2048 + 1024);
      u32x4 o;
      o[0] = pk_bf16(accA[8 * CS + 0] + s0[0], accA[8 * CS + 1] + s0[1]);
      o[1] = pk_bf16(accA[8 * CS + 2] + s0[2], accA[8 * CS + 3] + s0[3]);
      o[2] = pk_bf16(accA[8 * CS + 4] + s1[0], accA[8 * CS + 5] + s1[1]);
      o[3] = pk_bf16(accA[8 * CS + 6] + s1[2], accA[8 * CS + 7] + s1[3]);
      *(u32x4*)(dst + CS * ustep) = o;
    };
    if (cs == 0) tail(IC<0>{}); else tail(IC<1>{});
  } else {
    char* dst = dst0 + 5 * PSTEP;
#pragma unroll
    for (int u = 0; u < 2; ++u) {
      u32x4 o;
#pragma unroll
      for (int j = 0; j < 4; ++j)
        o[j] = GELU ? pk_bf16(gelu_tanh(accB[8 * u + 2 * j]), gelu_tanh(accB[8 * u + 2 * j + 1])) : pk_bf16(accB[8 * u + 2 * j], accB[8 * u + 2 * j + 1]);
      *(u32x4*)(dst + u * ustep) = o;
    }
  }
  WAIT_V(0);
  STAMP(ws, 0, 6);
}

__global__ __launch_bounds__(512, 2) void k0_inproj(const u16* __restrict__ x, const u16* __restrict__ wqkv,
                                                    const u16* __restrict__ w_in, const u16* __restrict__ w_sp,
                                                    const u16* __restrict__ w_out, const u16* __restrict__ wo, char* __restrict__ ws) {
  __shared__ __attribute__((aligned(16))) char lds[6 * K0_SLOT + 8192];
  const int b = blockIdx.x, xcd = b & 7, idx = b >> 3;
  const int cg = xcd * 2 + (idx & 1), rt = idx >> 1;
  if (cg < 6)       k0_body<1, 1, 0>(lds, x, wqkv, w_in, w_sp, w_out, wo, ws, rt, cg);
  else if (cg < 12) k0_body<0, 1, 1>(lds, x, wqkv, w_in, w_sp, w_out, wo, ws, rt, cg);
  else              k0_body<0, 0, 0>(lds, x, wqkv, w_in, w_sp, w_out, wo, ws, rt, cg);
}

constexpr int AT_TG = 320;
constexpr int AT_K = 0, AT_V = 64 * AT_TG, AT_Q = 2 * 64 * AT_TG, AT_ONES = AT_Q + 32 * AT_TG;
constexpr int K1_LDS = AT_ONES + 16;

DEVI void attn_role(char* lds, char* __restrict__ ws, int head, int qb) {
  const int tid = threadIdx.x, lane = tid & 63;
  const int w = __builtin_amdgcn_readfirstlane(tid >> 6);
  const int h = lane >> 5;
  const char* zq = ws + OFF_ZQ;
  const int tgk0 = 32 * qb - 32;
  for (int n = w; n < 50; n += 8) {
    int part, nn;
    if (n < 20) { part = 1; nn = n; } else if (n < 40) { part = 2; nn = n - 20; } else { part = 0; nn = n - 40; }
    int i = nn * 64 + lane;
    int tg = i / 20, d = i - tg * 20;
    d = d > 17 ? 17 : d;
    int gtg = (part == 0) ? 32 * qb + tg : tgk0 + tg;
    gtg = gtg < 0 ? 0 : gtg;
    const char* src = zq + ((size_t)gtg * NZQ + FH + 576 * part + 18 * head + d) * 16;
    char* dst = lds + (part == 1 ? AT_K : part == 2 ? AT_V : AT_Q) + nn * 1024;
    GLDS(src, dst);
  }
  if (tid == 0) { u32x4 o = {0x3f803f80u, 0x3f803f80u, 0x3f803f80u, 0x3f803f80u}; *(u32x4*)(lds + AT_ONES) = o; }
  WAIT_V(0);
  BAR();

  const int q4 = (lane & 15) >> 2, p4 = lane & 3, g16 = (lane >> 4) & 1;
  u32x4 qf0, qf1;
  {
    const char* qb_ = lds + AT_Q + (4 * w + 2 * g16 + (p4 >> 1)) * AT_TG + (p4 & 1) * 8;
    int d0 = 8 * h + q4;
    qf0 = tr_read2(qb_ + d0 * 16, qb_ + (d0 + 4) * 16);
    int d1 = 16 + q4; d1 = d1 > 17 ? 17 : d1;
    u32x2 t = tr_read(qb_ + d1 * 16);
    qf1[0] = h ? 0u : t[0]; qf1[1] = 0u; qf1[2] = 0u; qf1[3] = 0u;
  }
  const char* kb0 = lds + AT_K + (4 * w + 2 * g16 + (p4 & 1)) * AT_TG + (p4 >> 1) * 8;
  const int kd0 = (8 * h + q4) * 16;
  const int kd1 = (16 + q4 > 17 ? 17 : 16 + q4) * 16;
  const int n = lane & 31;
  const int jj0 = (qb == 0) ? (8 - w) : 0;
  const float NEG = -3.0e38f;

  auto scores = [&](int jj) -> f32x16 {
    const char* kb = kb0 + jj * 4 * AT_TG;
    u32x4 k0 = tr_read2(kb + kd0, kb + kd0 + 64);
    u32x2 k1a = tr_read(kb + kd1);
    u32x4 k1 = {k1a[0], k1a[1], k1a[0], k1a[1]};
    f32x16 s;
#pragma unroll
    for (int i = 0; i < 16; ++i) s[i] = 0.f;
    s = mfma32(k0, qf0, s);
    s = mfma32(k1, qf1, s);
    if (jj == 0) {
#pragma unroll
      for (int r = 0; r < 16; ++r) { int kap = (r & 7) + 8 * h + 16 * (r >> 3); s[r] = (kap > n) ? s[r] : NEG; }
    } else if (jj == 8) {
#pragma unroll
      for (int r = 0; r < 16; ++r) { int kap = (r & 7) + 8 * h + 16 * (r >> 3); s[r] = (kap <= n) ? s[r] : NEG; }
    }
    return s;
  };

  float m = NEG;
  for (int jj = jj0; jj <= 8; ++jj) {
    f32x16 s = scores(jj);
#pragma unroll
    for (int r = 0; r < 16; ++r) m = fmaxf(m, s[r]);
  }
  m = fmaxf(m, __shfl_xor(m, 32));
  const float c = 0.23570226039551584f * 1.4426950408889634f;
  const float mc = m * c;
  f32x16 o;
#pragma unroll
  for (int i = 0; i < 16; ++i) o[i] = 0.f;
  const int vd = lane & 31;
  const char* vb0 = (vd == 18) ? lds + AT_ONES : lds + AT_V + (4 * w + h) * AT_TG + vd * 16;
  const int vstep = (vd == 18) ? 0 : AT_TG;
  for (int jj = jj0; jj <= 8; ++jj) {
    f32x16 s = scores(jj);
    u32x4 p0, p1;
#pragma unroll
    for (int j = 0; j < 4; ++j) {
      p0[j] = pk_bf16(fexp2(__builtin_fmaf(s[2 * j], c, -mc)), fexp2(__builtin_fmaf(s[2 * j + 1], c, -mc)));
      p1[j] = pk_bf16(fexp2(__builtin_fmaf(s[8 + 2 * j], c, -mc)), fexp2(__builtin_fmaf(s[8 + 2 * j + 1], c, -mc)));
    }
    const char* vb = vb0 + jj * 4 * vstep;
    u32x4 v0 = *(const u32x4*)vb;
    u32x4 v1 = *(const u32x4*)(vb + 2 * vstep);
    o = mfma32(v0, p0, o);
    o = mfma32(v1, p1, o);
  }
  float l = o[10];
  float lo = __shfl_xor(l, 32);
  l = h ? lo : l;
  const float inv = 1.0f / l;
  const int t = 256 * qb + 32 * w + n;
  char* po = ws + OFF_PO + (size_t)t * 16;
  const int cb = 18 * head + 4 * h;
#pragma unroll
  for (int pr = 0; pr < 5; ++pr) {
    const int r = 2 * pr;
    const int d = (r & 3) + 8 * (r >> 2);
    if (pr < 4 || h == 0) {
      int cc = cb + d;
      u32 v = pk_bf16(o[r] * inv, o[r + 1] * inv);
      *(u32*)(po + (size_t)(288 + (cc >> 3)) * 2048 * 16 + (cc & 7) * 2) = v;
    }
  }
}

DEVI void z2n_role(char* lds, char* __restrict__ ws, int blk) {
  const int tid = threadIdx.x;
  const int half = tid >> 8, j = tid & 255, wv = tid >> 6;
  const int tg = blk * 2 + half;
  const char* src = ws + OFF_ZQ + ((size_t)tg * NZQ + j) * 16;
  u32x4 v[9];
#pragma unroll
  for (int i = 0; i < 9; ++i) v[i] = *(const u32x4*)(src + i * 4096);
  float* red = (float*)lds;
  float s[8];
#pragma unroll
  for (int e = 0; e < 8; ++e) s[e] = 0.f;
#pragma unroll
  for (int i = 0; i < 9; ++i) {
#pragma unroll
    for (int e = 0; e < 4; ++e) { s[2 * e] += bf_lo(v[i][e]); s[2 * e + 1] += bf_hi(v[i][e]); }
  }
#pragma unroll
  for (int e = 0; e < 8; ++e) {
#pragma unroll
    for (int o = 32; o >= 1; o >>= 1) s[e] += __shfl_xor(s[e], o);
  }
  if ((tid & 63) == 0) {
#pragma unroll
    for (int e = 0; e < 8; ++e) red[wv * 8 + e] = s[e];
  }
  __syncthreads();
  float mean[8];
#pragma unroll
  for (int e = 0; e < 8; ++e) {
    const float* r = red + half * 32 + e;
    mean[e] = (r[0] + r[8] + r[16] + r[24]) * (1.0f / 2304.0f);
    s[e] = 0.f;
  }
#pragma unroll
  for (int i = 0; i < 9; ++i) {
#pragma unroll
    for (int e = 0; e < 4; ++e) {
      float a = bf_lo(v[i][e]) - mean[2 * e], b = bf_hi(v[i][e]) - mean[2 * e + 1];
      s[2 * e] = __builtin_fmaf(a, a, s[2 * e]);
      s[2 * e + 1] = __builtin_fmaf(b, b, s[2 * e + 1]);
    }
  }
#pragma unroll
  for (int e = 0; e < 8; ++e) {
#pragma unroll
    for (int o = 32; o >= 1; o >>= 1) s[e] += __shfl_xor(s[e], o);
  }
  if ((tid & 63) == 0) {
#pragma unroll
    for (int e = 0; e < 8; ++e) red[64 + wv * 8 + e] = s[e];
  }
  __syncthreads();
  float rs[8], nb[8];
#pragma unroll
  for (int e = 0; e < 8; ++e) {
    const float* r = red + 64 + half * 32 + e;
    float var = (r[0] + r[8] + r[16] + r[24]) * (1.0f / 2304.0f);
    rs[e] = 1.0f / sqrtf(var + 1e-5f);
    nb[e] = -mean[e] * rs[e];
  }
  char* dst = ws + OFF_ZN + ((size_t)tg * FH + j) * 16;
#pragma unroll
  for (int i = 0; i < 9; ++i) {
    u32x4 o;
#pragma unroll
    for (int e = 0; e < 4; ++e)
      o[e] = pk_bf16(__builtin_fmaf(bf_lo(v[i][e]), rs[2 * e], nb[2 * e]),
                     __builtin_fmaf(bf_hi(v[i][e]), rs[2 * e + 1], nb[2 * e + 1]));
    *(u32x4*)(dst + i * 4096) = o;
  }
}

DEVI void tile_role(const u16* __restrict__ w_out, const u16* __restrict__ wo, char* __restrict__ ws, int blk) {
  const int g = blk * 512 + threadIdx.x;
  if (g >= 360 * 72) return;
  const int fc = g / 72, dq = g - fc * 72;
  const char* src = (fc < 288) ? (const char*)w_out + (size_t)fc * 8 * 1152 : (const char*)wo + (size_t)(fc - 288) * 8 * 1152;
  src += dq * 16;
  u32x4 r[8];
#pragma unroll
  for (int i = 0; i < 8; ++i) r[i] = *(const u32x4*)(src + i * 1152);
  char* dst = ws + OFF_WT + ((size_t)fc * 576 + dq * 8) * 16;
#pragma unroll
  for (int d = 0; d < 8; ++d) {
    u32x4 o;
#pragma unroll
    for (int m = 0; m < 4; ++m)
      o[m] = __builtin_amdgcn_perm(r[2 * m + 1][d >> 1], r[2 * m][d >> 1], (d & 1) ? 0x07060302u : 0x05040100u);
    *(u32x4*)(dst + d * 16) = o;
  }
}

__global__ __launch_bounds__(512, 4) void k1_mid(const u16* __restrict__ w_out, const u16* __restrict__ wo, char* __restrict__ ws) {
  __shared__ __attribute__((aligned(16))) char lds[K1_LDS];
  const int b = blockIdx.x;
  if (b < 256) {
    const int xcd = b & 7, idx = b >> 3;
    attn_role(lds, ws, xcd * 4 + (idx & 3), idx >> 2);
  } else if (b < 384) {
    z2n_role(lds, ws, b - 256);
  } else {
    tile_role(w_out, wo, ws, b - 384);
  }
}

constexpr int K2_SLOT = 16384 + 8 * 2304;

template <int EXTRA>
DEVI void k2_body(char* lds, const u16* __restrict__ wsp, const u16* __restrict__ bsp, char* __restrict__ ws) {
  const int tid = threadIdx.x, lane = tid & 63;
  const int w = __builtin_amdgcn_readfirstlane(tid >> 6);
  const int kh = w >> 2, tq = w & 3;
  const int b = blockIdx.x, xcd = b & 7, idx = b >> 3;
  const int tb = (xcd & 3) * 4 + (idx & 3), fb = (xcd >> 2) * 8 + (idx >> 2);
  const int t0 = tb * 128, f0 = fb * 144;
  STAMP(ws, 2, 0);

  u32 wv0, wv1, zv[3];
  {
    int r0 = 16 * w + (lane >> 3), r1 = r0 + 8;
    wv0 = (u32)(r0 * 4096 + (((lane & 7) ^ ((r0 >> 1) & 7)) * 16));
    wv1 = (u32)(r1 * 4096 + (((lane & 7) ^ ((r1 >> 1) & 7)) * 16));
#pragma unroll
    for (int j = 0; j < 3; ++j) {
      int e = (w + 8 * j) * 64 + lane;
      int tg = e / 144, f = e - tg * 144;
      zv[j] = (u32)((tg * FH + f) * 16);
    }
  }
  const srd_t wsrd = MAKE_SRD((const char*)wsp + (size_t)t0 * 4096);
  const srd_t zsrd = MAKE_SRD(ws + OFF_ZN + (size_t)f0 * 16);
  const u32 ldsb = LDS_ADDR(lds) + w * 1024;
  auto piece = [&](int it, int j) {
    const u32 sl = ldsb + (it & 3) * K2_SLOT;
    const u32 wo = it * 128, zo = it * (8 * FH * 16);
    if (j == 0) BUFL(sl + w * 1024, wv0, wsrd, wo);
    if (j == 1) BUFL(sl + 16384, zv[0], zsrd, zo);
    if (j == 2) BUFL(sl + w * 1024 + 1024, wv1, wsrd, wo);
    if (j == 3) BUFL(sl + 16384 + 8192, zv[1], zsrd, zo);
    if (j == 4 && EXTRA) BUFL(sl + 16384 + 16384, zv[2], zsrd, zo);
  };
  auto issue = [&](int it) { piece(it, 0); piece(it, 1); piece(it, 2); piece(it, 3); piece(it, 4); };
#define K2_WAIT(n) do { if (EXTRA) WAIT_V_(n * 5); else WAIT_V_(n * 4); } while (0)
#define K2_WAITZ(n) do { if (EXTRA) WAIT_V_(n * 5 + 12); else WAIT_V_(n * 4 + 12); } while (0)
  issue(0); issue(1); issue(2); issue(3);

  const int g = lane >> 4, l15 = lane & 15;
  f32x4 acc[9][2];
#pragma unroll
  for (int i = 0; i < 9; ++i) { acc[i][0] = f32x4{0.f, 0.f, 0.f, 0.f}; acc[i][1] = f32x4{0.f, 0.f, 0.f, 0.f}; }
  int woff[2];
#pragma unroll
  for (int ti = 0; ti < 2; ++ti) {
    int row = 32 * tq + 16 * ti + l15;
    woff[ti] = row * 128 + (((4 * kh + g) ^ ((row >> 1) & 7)) * 16);
  }
  const int zoff = 16384 + (4 * kh + g) * 2304 + l15 * 16;

  struct Frags { u32x4 a[9]; u32x4 b[2]; };
  auto rdb = [&](int it, Frags& F) {
    const char* sl = lds + (it & 3) * K2_SLOT;
    F.b[0] = *(const u32x4*)(sl + woff[0]);
    F.b[1] = *(const u32x4*)(sl + woff[1]);
  };
  auto rda = [&](int it, Frags& F, int fi) {
    const char* sl = lds + (it & 3) * K2_SLOT;
    F.a[fi] = *(const u32x4*)(sl + zoff + fi * 256);
  };
  auto mm1 = [&](const Frags& F, int fi) {
    acc[fi][0] = mfma16(F.a[fi], F.b[0], acc[fi][0]);
    acc[fi][1] = mfma16(F.a[fi], F.b[1], acc[fi][1]);
  };
  auto stage = [&](const Frags& C, Frags& N, int rit, int lit, auto LDc, auto RDc) {
    constexpr bool LD = decltype(LDc)::value, RD = decltype(RDc)::value;
    SB();
    if (RD) rdb(rit, N);
    mm1(C, 0); SB();
    if (RD) { rda(rit, N, 0); rda(rit, N, 1); }
    mm1(C, 1); if (LD) piece(lit, 0); SB();
    if (RD) { rda(rit, N, 2); rda(rit, N, 3); }
    mm1(C, 2); SB();
    if (RD) { rda(rit, N, 4); rda(rit, N, 5); }
    mm1(C, 3); if (LD) piece(lit, 1); SB();
    if (RD) { rda(rit, N, 6); rda(rit, N, 7); }
    mm1(C, 4); SB();
    if (RD) rda(rit, N, 8);
    mm1(C, 5); if (LD) piece(lit, 2); SB();
    mm1(C, 6); SB();
    mm1(C, 7); if (LD) piece(lit, 3); SB();
    mm1(C, 8); if (LD) piece(lit, 4); SB();
  };
  using LDT = std::true_type;
  using T = std::true_type; using Fa = std::false_type;
  Frags F0, F1;
  K2_WAIT(3); BAR();
  STAMP(ws, 2, 1);
  rdb(0, F0);
#pragma unroll
  for (int fi = 0; fi < 9; ++fi) rda(0, F0, fi);
  for (int it = 0; it < 28; it += 2) {
    K2_WAIT(2); BAR();
    stage(F0, F1, it + 1, it + 4, LDT{}, T{});
    K2_WAIT(2); BAR();
    stage(F1, F0, it + 2, it + 5, LDT{}, T{});
  }
  STAMP(ws, 2, 2);
  const size_t eoff = ((size_t)(f0 / 8 + (g >> 1)) * 2048 + t0 + 32 * tq + l15) * 16 + (g & 1) * 8;
  u32x2 z1v[5][2];
  u32 biasr[2];
  K2_WAIT(2); BAR();
#pragma unroll
  for (int fi = 0; fi < 5; ++fi) {
    const int fa = (kh == 0) ? fi : (fi < 4 ? 5 + fi : 8);
#pragma unroll
    for (int ti = 0; ti < 2; ++ti)
      z1v[fi][ti] = *(const u32x2*)(ws + OFF_Z1 + eoff + ((size_t)fa * 2 * 2048 + ti * 16) * 16);
  }
#pragma unroll
  for (int ti = 0; ti < 2; ++ti) biasr[ti] = (u32)bsp[t0 + 32 * tq + 16 * ti + l15];
  stage(F0, F1, 29, 0, Fa{}, T{});
  K2_WAITZ(1); BAR(); stage(F1, F0, 30, 0, Fa{}, T{});
  K2_WAITZ(0); BAR(); stage(F0, F1, 31, 0, Fa{}, T{});
  stage(F1, F0, 0, 0, Fa{}, Fa{});
  STAMP(ws, 2, 3);
  float bias[2] = {bf_lo(biasr[0]), bf_lo(biasr[1])};
  BAR();
  char* xch = lds + tq * 18432 + lane * 16;
  auto finish = [&](auto KH) {
    constexpr int khc = decltype(KH)::value;
    constexpr int lo = khc ? 5 : 0, hi = khc ? 9 : 5;
#pragma unroll
    for (int fi = 0; fi < 9; ++fi)
      if (fi < lo || fi >= hi) {
        *(f32x4*)(xch + (fi * 2 + 0) * 1024) = acc[fi][0];
        *(f32x4*)(xch + (fi * 2 + 1) * 1024) = acc[fi][1];
      }
    BAR();
#pragma unroll
    for (int fi = lo; fi < hi; ++fi)
#pragma unroll
      for (int ti = 0; ti < 2; ++ti) {
        f32x4 o = *(const f32x4*)(xch + (fi * 2 + ti) * 1024);
        f32x4 gq = acc[fi][ti] + o;
        u32x2 z = z1v[fi - lo][ti];
        u32x2 r;
        r[0] = pk_bf16(bf_lo(z[0]) * (gq[0] + bias[ti]), bf_hi(z[0]) * (gq[1] + bias[ti]));
        r[1] = pk_bf16(bf_lo(z[1]) * (gq[2] + bias[ti]), bf_hi(z[1]) * (gq[3] + bias[ti]));
        *(u32x2*)(ws + OFF_PO + eoff + ((size_t)fi * 2 * 2048 + ti * 16) * 16) = r;
      }
  };
  if (kh == 0) finish(std::integral_constant<int, 0>{});
  else         finish(std::integral_constant<int, 1>{});
  STAMP(ws, 2, 4);
}

__global__ __launch_bounds__(512, 2) void k2_spatial(const u16* __restrict__ wsp, const u16* __restrict__ bsp, char* __restrict__ ws) {
  __shared__ __attribute__((aligned(16))) char lds[4 * K2_SLOT];
  if (__builtin_amdgcn_readfirstlane(threadIdx.x >> 6) < 2) k2_body<1>(lds, wsp, bsp, ws);
  else                   k2_body<0>(lds, wsp, bsp, ws);
}

constexpr int K3_SLOT = 8192 + 9216;

template <int EXTRA>
DEVI void k3_body(char* lds, const u16* __restrict__ x, char* __restrict__ ws, u16* __restrict__ out) {
  const int tid = threadIdx.x, lane = tid & 63;
  const int w = __builtin_amdgcn_readfirstlane(tid >> 6);
  const int kh = w >> 2, th = (w >> 1) & 1, dh = (w & 1) ^ kh;
  const int b = blockIdx.x, xcd = b & 7, idx = b >> 3;
  const int tb = xcd * 4 + (idx & 3), db = idx >> 2;
  const int t0 = tb * 64, d0 = db * 72;
  STAMP(ws, 3, 0);

  u32 pv = (u32)((w * 2048 + lane) * 16), wv[2];
#pragma unroll
  for (int j = 0; j < 2; ++j) {
    int e = (w + 8 * j) * 64 + lane;
    int fc = e / 72, dp = e - fc * 72;
    int d = ((fc & 1) && dp < 64) ? (dp ^ 8) : dp;
    wv[j] = (u32)((fc * 576 + d) * 16);
  }
  const srd_t psrd = MAKE_SRD(ws + OFF_PO + (size_t)t0 * 16);
  const srd_t wsrd = MAKE_SRD(ws + OFF_WT + (size_t)d0 * 16);
  const u32 ldsb = LDS_ADDR(lds) + w * 1024;
  auto piece = [&](int it, int j) {
    const u32 sl = ldsb + (it & 7) * K3_SLOT;
    if (j == 0) BUFL(sl, pv, psrd, it * (8 * 2048 * 16));
    if (j == 1) BUFL(sl + 8192, wv[0], wsrd, it * (8 * 576 * 16));
    if (j == 2 && EXTRA) BUFL(sl + 8192 + 8192, wv[1], wsrd, it * (8 * 576 * 16));
  };
  auto issue = [&](int it) { piece(it, 0); piece(it, 1); piece(it, 2); };
#define K3_WAIT(n, z) do { if (EXTRA) WAIT_V_((n) * 3 + (z)); else WAIT_V_((n) * 2 + (z)); } while (0)
#pragma unroll
  for (int i = 0; i < 8; ++i) issue(i);

  const int g = lane >> 4, l15 = lane & 15;
  const int poff = (4 * kh + g) * 1024 + (32 * th + l15) * 16;
  int aoff[3];
#pragma unroll
  for (int di = 0; di < 3; ++di) {
    int d = 48 * dh + 16 * di + l15;
    if (d >= 72) d = 64 + (l15 & 7);
    int dp = ((g & 1) && d < 64) ? (d ^ 8) : d;
    aoff[di] = 8192 + ((4 * kh + g) * 72 + dp) * 16;
  }
  f32x4 accB[3][2], accS[3][2];
#pragma unroll
  for (int i = 0; i < 3; ++i)
#pragma unroll
    for (int j = 0; j < 2; ++j) { accB[i][j] = f32x4{0.f, 0.f, 0.f, 0.f}; accS[i][j] = f32x4{0.f, 0.f, 0.f, 0.f}; }

  struct Frags { u32x4 a[3]; u32x4 b[2]; };
  auto body = [&](auto DHc) {
    constexpr int DH = decltype(DHc)::value;
    constexpr int NA = DH ? 2 : 3;
    auto rdb = [&](int it, Frags& F) {
      const char* sl = lds + (it & 7) * K3_SLOT;
      F.b[0] = *(const u32x4*)(sl + poff);
      F.b[1] = *(const u32x4*)(sl + poff + 256);
    };
    auto rda = [&](int it, Frags& F) {
      const char* sl = lds + (it & 7) * K3_SLOT;
#pragma unroll
      for (int di = 0; di < NA; ++di) F.a[di] = *(const u32x4*)(sl + aoff[di]);
    };
    auto mm1 = [&](const Frags& F, f32x4 (&acc)[3][2], int di) {
      acc[di][0] = mfma16(F.a[di], F.b[0], acc[di][0]);
      acc[di][1] = mfma16(F.a[di], F.b[1], acc[di][1]);
    };
    auto stage = [&](const Frags& C, Frags& N, f32x4 (&acc)[3][2], int rit, int lit, auto LDc, auto RDc) {
      constexpr bool LD = decltype(LDc)::value, RD = decltype(RDc)::value;
      SB();
      if (RD) rdb(rit, N);
      mm1(C, acc, 0); if (LD) piece(lit, 0); SB();
      if (RD) rda(rit, N);
      mm1(C, acc, 1); if (LD) piece(lit, 1); SB();
      if (NA == 3) mm1(C, acc, 2);
      if (LD) piece(lit, 2);
      SB();
    };
    using LDT = std::true_type;
    using T = std::true_type; using Fa = std::false_type;
    Frags F0, F1;
    K3_WAIT(7, 0); BAR();
    STAMP(ws, 3, 1);
    rdb(0, F0); rda(0, F0);
    for (int it = 0; it < 36; it += 2) {
      K3_WAIT(6, 0); BAR();
      stage(F0, F1, accB, it + 1, it + 8, LDT{}, T{});
      K3_WAIT(6, 0); BAR();
      stage(F1, F0, accB, it + 2, it + 9, LDT{}, T{});
    }
    STAMP(ws, 3, 2);
    K3_WAIT(6, 0); BAR();
    stage(F0, F1, accS, 37, 44, LDT{}, T{});
    const int t = t0 + 32 * th + 16 * kh + l15;
    u32x2 xv[3];
#pragma unroll
    for (int di = 0; di < 3; ++di) {
      int d = 48 * dh + 16 * di + 4 * g;
      d = d < 72 ? d : 0;
      xv[di] = *(const u32x2*)(x + (size_t)t * 576 + d0 + d);
    }
    K3_WAIT(6, 3); BAR(); stage(F1, F0, accS, 38, 0, Fa{}, T{});
    K3_WAIT(5, 3); BAR(); stage(F0, F1, accS, 39, 0, Fa{}, T{});
    K3_WAIT(4, 3); BAR(); stage(F1, F0, accS, 40, 0, Fa{}, T{});
    K3_WAIT(3, 3); BAR(); stage(F0, F1, accS, 41, 0, Fa{}, T{});
    K3_WAIT(2, 3); BAR(); stage(F1, F0, accS, 42, 0, Fa{}, T{});
    K3_WAIT(1, 3); BAR(); stage(F0, F1, accS, 43, 0, Fa{}, T{});
    K3_WAIT(0, 3); BAR(); stage(F1, F0, accS, 44, 0, Fa{}, T{});
    stage(F0, F1, accS, 0, 0, Fa{}, Fa{});
    STAMP(ws, 3, 3);
    BAR();
    char* xch = lds + ((th * 2 + dh) * 2) * 6144 + lane * 16;
    auto finish = [&](auto KH) {
      constexpr int own = decltype(KH)::value, oth = own ^ 1;
#pragma unroll
      for (int di = 0; di < NA; ++di) {
        *(f32x4*)(xch + oth * 6144 + di * 1024) = accB[di][oth];
        *(f32x4*)(xch + oth * 6144 + 3072 + di * 1024) = accS[di][oth];
      }
      BAR();
#pragma unroll
      for (int di = 0; di < NA; ++di) {
        f32x4 Bv = accB[di][own] + *(const f32x4*)(xch + own * 6144 + di * 1024);
        f32x4 Sv = accS[di][own] + *(const f32x4*)(xch + own * 6144 + 3072 + di * 1024);
        int d = 48 * dh + 16 * di + 4 * g;
        if (d < 72) {
          u32x2 r;
          r[0] = pk_bf16(bf_lo(xv[di][0]) + Sv[0] * silu_f(Bv[0]), bf_hi(xv[di][0]) + Sv[1] * silu_f(Bv[1]));
          r[1] = pk_bf16(bf_lo(xv[di][1]) + Sv[2] * silu_f(Bv[2]), bf_hi(xv[di][1]) + Sv[3] * silu_f(Bv[3]));
          *(u32x2*)(out + (size_t)t * 576 + d0 + d) = r;
        }
      }
    };
    if (kh == 0) finish(std::integral_constant<int, 0>{});
    else         finish(std::integral_constant<int, 1>{});
    STAMP(ws, 3, 4);
  };
  if (dh == 0) body(std::integral_constant<int, 0>{});
  else         body(std::integral_constant<int, 1>{});
}

__global__ __launch_bounds__(512, 2) void k3_out(const u16* __restrict__ x, char* __restrict__ ws, u16* __restrict__ out) {
  __shared__ __attribute__((aligned(16))) char lds[8 * K3_SLOT];
  if (__builtin_amdgcn_readfirstlane(threadIdx.x >> 6) == 0) k3_body<1>(lds, x, ws, out);
  else                                                       k3_body<0>(lds, x, ws, out);
}

__global__ void k_empty(char* ws) { if (ws == nullptr) ws[0] = 1; }
static inline void launch_stage(int st, void* const* d_in, void* d_out, void* d_ws, hipStream_t stream) {
  const u16* x = (const u16*)d_in[0];
  const u16* wqkv = (const u16*)d_in[1];
  const u16* wo = (const u16*)d_in[2];
  const u16* w_in = (const u16*)d_in[4];
  const u16* w_sp = (const u16*)d_in[5];
  const u16* b_sp = (const u16*)d_in[6];
  const u16* w_out = (const u16*)d_in[7];
  char* ws = (char*)d_ws;
  if (st == 0) k0_inproj<<<256, 512, 0, stream>>>(x, wqkv, w_in, w_sp, w_out, wo, ws);
  if (st == 1) k1_mid<<<384 + 51, 512, 0, stream>>>(w_out, wo, ws);
  if (st == 2) k2_spatial<<<256, 512, 0, stream>>>(w_sp, b_sp, ws);
  if (st == 4) k_empty<<<256, 512, 0, stream>>>(ws);
  if (st == 3) k3_out<<<256, 512, 0, stream>>>(x, ws, (u16*)d_out);
}

extern "C" void kernel_launch(void* const* d_in, const int* in_sizes, int n_in, void* d_out, int out_size, void* d_ws,
                              size_t ws_size, hipStream_t stream) {
  (void)in_sizes; (void)n_in; (void)out_size; (void)ws_size;
  for (int st = 0; st < 4; ++st) launch_stage(st, d_in, d_out, d_ws, stream);
}
```

```cpp
#define AUX3 0
#include <hip/hip_runtime.h>
#include <hip/hip_bf16.h>
#include <stdint.h>
#include <type_traits>

typedef unsigned int u32;
typedef unsigned short u16;
typedef __attribute__((ext_vector_type(8))) __bf16 bf16x8;
typedef __attribute__((ext_vector_type(2))) __bf16 bf16x2;
typedef __attribute__((ext_vector_type(4))) short s16x4;
typedef __attribute__((ext_vector_type(2))) float f32x2;
typedef __attribute__((ext_vector_type(4))) float f32x4;
typedef __attribute__((ext_vector_type(16))) float f32x16;
typedef __attribute__((ext_vector_type(2))) u32 u32x2;
typedef __attribute__((ext_vector_type(4))) u32 u32x4;

#define DEVI __device__ __forceinline__
#define AS1 __attribute__((address_space(1)))
#define AS3 __attribute__((address_space(3)))
#define GLDS(g, l) __builtin_amdgcn_global_load_lds((const AS1 void*)(g), (AS3 void*)(l), 16, 0, 0)
#define WAIT_V(n) asm volatile("s_waitcnt vmcnt(" #n ")" ::: "memory")
#define WAIT_V_(n) asm volatile("s_waitcnt vmcnt(%0)" ::"n"(n) : "memory")
#define SB() __builtin_amdgcn_sched_barrier(0)
typedef __amdgpu_buffer_rsrc_t srd_t;
#define BUFL(m0v, voff, srd, soff) \
  asm volatile("s_mov_b32 m0, %0\n\ts_nop 0\n\tbuffer_load_dwordx4 %1, %2, %3 offen lds" ::"s"(m0v), "v"(voff), "s"(srd), "s"(soff) : "m0", "memory")
#define MAKE_SRD(p) __builtin_amdgcn_make_buffer_rsrc((void*)(p), (short)0, 0x7fffffff, 0x00020000)
#define LDS_ADDR(p) ((u32)(uintptr_t)(AS3 char*)(p))
#define WAIT_L0() asm volatile("s_waitcnt lgkmcnt(0)" ::: "memory")
#define SGB(m, n) __builtin_amdgcn_sched_group_barrier(m, n, 0)
#define BAR() do { WAIT_L0(); __builtin_amdgcn_s_barrier(); asm volatile("" ::: "memory"); } while (0)

#define AUX0 16
#define AUX1 16
#define AUX2 16
template <int AUX> DEVI void st128(srd_t r, const char* base, const char* p, u32x4 v) { __builtin_amdgcn_raw_buffer_store_b128(v, r, (u32)(p - base), 0, AUX); }
template <int AUX> DEVI void st64(srd_t r, const char* base, const char* p, u32x2 v) { __builtin_amdgcn_raw_buffer_store_b64(v, r, (u32)(p - base), 0, AUX); }
template <int AUX> DEVI void st32(srd_t r, const char* base, const char* p, u32 v) { __builtin_amdgcn_raw_buffer_store_b32(v, r, (u32)(p - base), 0, AUX); }

constexpr int S_ = 2048, D_ = 576, FH = 2304, NZQ = 4032;
constexpr size_t OFF_Z1 = 0;
constexpr size_t OFF_ZQ = OFF_Z1 + (size_t)288 * 2048 * 16;
constexpr size_t OFF_ZN = OFF_ZQ + (size_t)256 * NZQ * 16;
constexpr size_t OFF_PO = OFF_ZN + (size_t)256 * FH * 16;
constexpr size_t OFF_WT = OFF_PO + (size_t)360 * 2048 * 16;
constexpr size_t WS_END = OFF_WT + (size_t)360 * 576 * 16;

#define STAMP(ws, k, i)
DEVI float bf_lo(u32 v) { return __builtin_bit_cast(float, v << 16); }
DEVI float bf_hi(u32 v) { return __builtin_bit_cast(float, v & 0xffff0000u); }
DEVI u32 pk_bf16(float a, float b) {
  f32x2 v = {a, b};
  return __builtin_bit_cast(u32, __builtin_convertvector(v, bf16x2));
}
DEVI f32x4 mfma16(u32x4 a, u32x4 b, f32x4 c) {
  return __builtin_amdgcn_mfma_f32_16x16x32_bf16(__builtin_bit_cast(bf16x8, a), __builtin_bit_cast(bf16x8, b), c, 0, 0, 0);
}
DEVI f32x16 mfma32(u32x4 a, u32x4 b, f32x16 c) {
  return __builtin_amdgcn_mfma_f32_32x32x16_bf16(__builtin_bit_cast(bf16x8, a), __builtin_bit_cast(bf16x8, b), c, 0, 0, 0);
}
DEVI u32x2 tr_read(const char* p) {
  s16x4 v = __builtin_amdgcn_ds_read_tr16_b64_v4i16((AS3 s16x4*)p);
  return __builtin_bit_cast(u32x2, v);
}
DEVI u32x4 tr_read2(const char* p0, const char* p1) {
  u32x2 a = tr_read(p0), b = tr_read(p1);
  u32x4 r = {a[0], a[1], b[0], b[1]};
  return r;
}
DEVI float fexp2(float x) { return __builtin_amdgcn_exp2f(x); }
DEVI float frcp(float x) { return __builtin_amdgcn_rcpf(x); }
DEVI float gelu_tanh(float v) {
  const float c0 = -2.0f * 0.7978845608028654f * 1.4426950408889634f;
  const float c1 = c0 * 0.044715f;
  float a = __builtin_fmaf(v * v, c1, c0);
  float e = fexp2(v * a);
  return v * frcp(1.0f + e);
}
DEVI float silu_f(float v) { return v * frcp(1.0f + fexp2(v * -1.4426950408889634f)); }
DEVI int swap23(int i) { return (i & ~12) | ((i & 4) << 1) | ((i & 8) >> 1); }

constexpr int K0_SLOT = 24576;
#define MAKE_SRDN(p, n) __builtin_amdgcn_make_buffer_rsrc((void*)(p), (short)0, (int)(n), 0x00020000)
template <int V> using IC = std::integral_constant<int, V>;
DEVI void bufl(u32 m0v, u32 voff, srd_t srd, u32 soff) { BUFL(m0v, voff, srd, soff); }
DEVI float dpp_xor1(float v) { return __builtin_bit_cast(float, __builtin_amdgcn_update_dpp(0, __builtin_bit_cast(int, v), 0xB1, 0xf, 0xf, true)); }
DEVI float dpp_xor2(float v) { return __builtin_bit_cast(float, __builtin_amdgcn_update_dpp(0, __builtin_bit_cast(int, v), 0x4E, 0xf, 0xf, true)); }

template <int ZORI, int GELU, int NP7>
DEVI void k0_body(char* lds, const u16* __restrict__ x, const u16* __restrict__ wqkv, const u16* __restrict__ w_in,
                  const u16* __restrict__ w_sp, const u16* __restrict__ w_out, const u16* __restrict__ wo,
                  char* __restrict__ ws, int rt, int cg) {
  const int tid = threadIdx.x, lane = tid & 63;
  const int w = __builtin_amdgcn_readfirstlane(tid >> 6);
  const int tgp = w & 3, cs = w >> 2;
  const int h = lane >> 5;
  const int t0 = rt * 128;
  const int tok = swap23(lane & 31);
  constexpr int NPAIR = 6 + NP7;
  constexpr bool IS_Q = !GELU;
  STAMP(ws, 0, 0);

  u32x4 xr[18];
  const int xrow = 16 * w + (lane >> 2), xj = lane & 3;
  {
    const char* xp = (const char*)x + (size_t)(t0 + xrow) * 1152 + xj * 16;
#pragma unroll
    for (int i = 0; i < 18; ++i) xr[i] = *(const u32x4*)(xp + i * 64);
  }
  constexpr int WSTR = IS_Q ? 3456 : 9216;
  const int wrow = 24 * w + (lane >> 3);
  const int wch = (lane & 7) ^ (((lane >> 4) & 1) << 2);
  const srd_t srdW = MAKE_SRD(IS_Q ? (const char*)wqkv + (cg - 12) * 768 : (const char*)w_in + cg * 768);
  const u32 voffW = wrow * WSTR + wch * 16;
  const int single = 48 + (cg - 6);
  const srd_t srdS = MAKE_SRD((const char*)wqkv + (NP7 ? (single >> 1) * 128 : 0));
  const u32 voffS = wrow * 3456 + wch * 16;
  const u32 ldsw = LDS_ADDR(lds) + w * 3072;
  const srd_t srdP0 = MAKE_SRD((const char*)w_sp + (size_t)blockIdx.x * 32768);
  const srd_t srdZ = MAKE_SRDN(x, 0);
  const srd_t srdO = MAKE_SRD(ws);
  const u32 pidx = blockIdx.x * 8 + w;
  const u32 vl16 = lane * 16;
  auto issue = [&](auto PQ_, auto TH_, auto SL_, auto N_) {
    constexpr int pq = PQ_.value, TH = TH_.value, SL = SL_.value, N = N_.value;
    const u32 m0v = ldsw + SL * K0_SLOT + N * 1024;
    if constexpr (pq < 6) {
      bufl(m0v, voffW, srdW, (u32)(pq * 128 + (TH * 192 + 8 * N) * WSTR));
    } else if constexpr (NP7 && pq == 6) {
      bufl(m0v, voffS, srdS, (u32)((TH * 192 + 8 * N) * 3456));
    } else {
      bufl(m0v, vl16, srdZ, 0u);
    }
  };
  auto pf = [&](auto I_) {
    constexpr int i = I_.value;
    const u32 m0v = LDS_ADDR(lds) + 6 * K0_SLOT + w * 1024;
    if constexpr (i < 4) {
      bufl(m0v, vl16, srdP0, (w * 4 + i) * 1024);
    } else if constexpr (i < 6) {
      const u32 c = pidx * 2 + (i - 4);
      const bool ok = c < 2592;
      bufl(m0v, vl16, MAKE_SRDN(w_out, ok ? FH * D_ * 2 : 0), ok ? c * 1024 : 0u);
    } else if constexpr (i == 6) {
      const bool ok = pidx < 648;
      bufl(m0v, vl16, MAKE_SRDN(wo, ok ? D_ * D_ * 2 : 0), ok ? pidx * 1024 : 0u);
    }
  };
  auto issue3 = [&](auto pq, auto TH_, auto SL_) { issue(pq, TH_, SL_, IC<0>{}); issue(pq, TH_, SL_, IC<1>{}); issue(pq, TH_, SL_, IC<2>{}); };

  u32x4 xa[36];
  {
    float s1 = 0.f, s2 = 0.f;
    const bf16x2 ones = __builtin_bit_cast(bf16x2, 0x3f803f80u);
#pragma unroll
    for (int i = 0; i < 18; ++i) {
#pragma unroll
      for (int j = 0; j < 4; ++j) {
        const u32 xv = xr[i][j];
        bf16x2 v = __builtin_bit_cast(bf16x2, xv);
        s1 = __builtin_amdgcn_fdot2_f32_bf16(v, ones, s1, false);
        s2 = __builtin_amdgcn_fdot2_f32_bf16(v, v, s2, false);
      }
    }
    s1 += dpp_xor1(s1); s2 += dpp_xor1(s2);
    s1 += dpp_xor2(s1); s2 += dpp_xor2(s2);
    const float mean = s1 * (1.0f / 576.0f);
    const float var = fmaxf(s2 * (1.0f / 576.0f) - mean * mean, 0.f);
    const float rstd = __builtin_amdgcn_rsqf(var + 1e-5f);
    const float nb = -mean * rstd;
    const f32x2 r2 = {rstd, rstd}, n2 = {nb, nb};
    STAMP(ws, 0, 1);
    auto norm = [&](int i) {
      f32x2 f[4];
#pragma unroll
      for (int j = 0; j < 4; ++j) { const u32 v = xr[i][j]; f[j][0] = bf_lo(v); f[j][1] = bf_hi(v); }
#pragma unroll
      for (int j = 0; j < 4; ++j) f[j] = __builtin_elementwise_fma(f[j], r2, n2);
      u32x4 o;
#pragma unroll
      for (int j = 0; j < 4; ++j) o[j] = pk_bf16(f[j][0], f[j][1]);
      return o;
    };
    const int fw = (xrow >> 1) & 7;
    char* wA = lds + xrow * 384 + ((xj ^ fw) * 16);
    char* wB = lds + xrow * 384 + (((4 + xj) ^ fw) * 16);
    SB();
    auto nw = [&](auto I_) {
      constexpr int I = I_.value, th = I / 6, m = I % 6;
      *(u32x4*)(((m & 1) ? wB : wA) + th * 49152 + (m >> 1) * 128) = norm(I);
      if constexpr (I & 1) issue(IC<0>{}, IC<(I / 6)>{}, IC<4 + I / 6>{}, IC<(I >> 1) % 3>{});
      SB();
    };
    nw(IC<0>{}); nw(IC<1>{}); nw(IC<2>{}); nw(IC<3>{}); nw(IC<4>{}); nw(IC<5>{});
    nw(IC<6>{}); nw(IC<7>{}); nw(IC<8>{}); nw(IC<9>{}); nw(IC<10>{}); nw(IC<11>{});
    const int row = 32 * tgp + tok;
    const int f = (row >> 1) & 7;
    const char* rb = lds + row * 384;
#define RDX(TH, BASE)                                                                    \
    _Pragma("unroll") for (int s = 0; s < 12; ++s) {                                     \
      int c = 2 * s + h;                                                                 \
      int cp = (c & ~7) | ((c & 7) ^ f);                                                 \
      xa[TH * 12 + s] = *(const u32x4*)(rb + BASE + cp * 16);                            \
    }
    STAMP(ws, 0, 2);
    BAR();
    RDX(0, 0)
    RDX(1, 49152)
    SB();
#pragma unroll
    for (int i = 12; i < 18; ++i) xr[i] = norm(i);
    SB();
    BAR();
    STAMP(ws, 0, 3);
#pragma unroll
    for (int i = 12; i < 18; ++i) {
      const int m = i - 12;
      *(u32x4*)(((m & 1) ? wB : wA) + 49152 + (m >> 1) * 128) = xr[i];
    }
    BAR();
    RDX(2, 49152)
    SB();
    issue3(IC<0>{}, IC<2>{}, IC<0>{});
    issue3(IC<1>{}, IC<0>{}, IC<1>{});
    SB();
    WAIT_L0();
#undef RDX
  }
  STAMP(ws, 0, 4);

  const int q4 = (lane & 15) >> 2, p4 = lane & 3, g16 = (lane >> 4) & 1;
  const int pp = ZORI ? (((p4 & 1) << 1) | (p4 >> 1)) : p4;
  const int rd_off = (8 * h + q4) * 128 + (((4 * cs + 2 * g16 + (pp >> 1)) ^ ((q4 >> 1) << 2)) * 16) + (pp & 1) * 8;
  u32 rdb0 = LDS_ADDR(lds) + rd_off, rdb1 = rdb0 + 2 * K0_SLOT, rdb2 = rdb0 + 4 * K0_SLOT;
  asm volatile("" : "+v"(rdb0), "+v"(rdb1), "+v"(rdb2));
  char* dst0;
  size_t ustep;
  constexpr size_t PSTEP = ZORI ? (size_t)8 * 2048 * 16 : (size_t)64 * 16;
  if (ZORI) {
    dst0 = ws + OFF_Z1 + ((size_t)((cg * 384 + cs * 32) / 8 + h) * 2048 + t0 + 32 * tgp + tok) * 16;
    ustep = (size_t)2 * 2048 * 16;
  } else {
    const int col = (IS_Q ? FH + (cg - 12) * 384 : (cg - 6) * 384) + cs * 32 + (lane & 31);
    dst0 = ws + OFF_ZQ + ((size_t)(t0 / 8 + 4 * tgp + h) * NZQ + col) * 16;
    ustep = (size_t)2 * NZQ * 16;
  }
  u32x4 wf[4];
  auto rdf = [&](int slot, int s) {
    const u32 a = (slot < 2 ? rdb0 : slot < 4 ? rdb1 : rdb2) + (slot & 1) * K0_SLOT + s * 2048;
    const u32x2 lo = __builtin_bit_cast(u32x2, __builtin_amdgcn_ds_read_tr16_b64_v4i16((AS3 s16x4*)(uintptr_t)a));
    const u32x2 hi = __builtin_bit_cast(u32x2, __builtin_amdgcn_ds_read_tr16_b64_v4i16((AS3 s16x4*)(uintptr_t)(a + 512)));
    const u32x4 r = {lo[0], lo[1], hi[0], hi[1]};
    return r;
  };
  f32x2 ee;
  u32x4 eo;
  auto piece = [&](auto K_, const f32x16& pv, char* dst) {
    constexpr int K = K_.value, J = K >> 1;
    const f32x2 v = {pv[2 * J], pv[2 * J + 1]};
    if constexpr ((K & 1) == 0) {
      if (GELU) {
        const float c0 = -2.0f * 0.7978845608028654f * 1.4426950408889634f;
        const float c1 = c0 * 0.044715f;
        const f32x2 c0v = {c0, c0}, c1v = {c1, c1};
        f32x2 a = __builtin_elementwise_fma(v * v, c1v, c0v);
        f32x2 m = v * a;
        ee[0] = fexp2(m[0]); ee[1] = fexp2(m[1]);
      }
    } else {
      if (GELU) {
        const f32x2 one = {1.f, 1.f};
        f32x2 d = ee + one;
        f32x2 r = {frcp(d[0]), frcp(d[1])};
        f32x2 o = v * r;
        eo[J & 3] = pk_bf16(o[0], o[1]);
      } else {
        eo[J & 3] = pk_bf16(v[0], v[1]);
      }
      if ((J & 3) == 3) st128<AUX0>(srdO, ws, dst + (J >> 2) * ustep, eo);
    }
  };
  auto pair = [&](auto P_, auto SPL_, f32x16& acc, const f32x16& pv) {
    constexpr int p = P_.value, ODD = p & 1, PREV = p > 0, SPL = SPL_.value;
    constexpr int NS = SPL ? 6 : 12, S0 = SPL ? 6 * (SPL - 1) : 0;
    char* dst = dst0 + (size_t)(p - 1) * PSTEP;
#pragma unroll
    for (int i = 0; i < 16; ++i) acc[i] = 0.f;
    auto stage = [&](auto TH_) {
      constexpr int TH = TH_.value;
      constexpr int SL = (3 * ODD + TH + 4) % 6, SN = (SL + 1) % 6, SI = (SL + 5) % 6;
      constexpr int ITH = (TH + 2) % 3;
      constexpr bool FIRST = !PREV && TH == 0;
      constexpr int ip = p + (TH == 0 ? 1 : 2);
      if (FIRST || (SPL && TH == 1)) WAIT_V(6); else if (!(SPL && TH == 2)) WAIT_V(9);
      __builtin_amdgcn_s_barrier();
      SB();
      if ((!PREV || SPL) && TH == 0) { wf[0] = rdf(SL, S0); wf[1] = rdf(SL, S0 + 1); wf[2] = rdf(SL, S0 + 2); SB(); }
      auto step = [&](auto J_) {
        constexpr int J = J_.value, S = S0 + J, G = TH * NS + J;
        if constexpr (J < NS) {
          if (ZORI) acc = mfma32(wf[G & 3], xa[TH * 12 + S], acc);
          else      acc = mfma32(xa[TH * 12 + S], wf[G & 3], acc);
          SB();
          if (!(SPL && TH == 2 && J + 3 >= NS)) wf[(G + 3) & 3] = (J + 3 < NS) ? rdf(SL, S + 3) : rdf(SN, S0 + J + 3 - NS);
          if constexpr (!SPL) {
            if constexpr (FIRST && (S == 0 || S == 2 || S == 4)) issue(IC<1>{}, IC<1>{}, IC<2>{}, IC<S / 2>{});
            if constexpr (S == (FIRST ? 6 : 1)) issue(IC<ip>{}, IC<ITH>{}, IC<SI>{}, IC<0>{});
            if constexpr (S == (FIRST ? 8 : 5)) issue(IC<ip>{}, IC<ITH>{}, IC<SI>{}, IC<1>{});
            if constexpr (S == (FIRST ? 10 : 9)) issue(IC<ip>{}, IC<ITH>{}, IC<SI>{}, IC<2>{});
            if constexpr (S == 11 && PREV) pf(IC<(p - 1) * 3 + TH>{});
          }
          if constexpr (PREV && !SPL && (G & 1) && G < 32) piece(IC<(G >> 1)>{}, pv, dst);
          if constexpr (PREV && SPL && G < 16) piece(IC<G>{}, pv, dst);
          SB();
        }
      };
      step(IC<0>{}); step(IC<1>{}); step(IC<2>{}); step(IC<3>{}); step(IC<4>{}); step(IC<5>{});
      step(IC<6>{}); step(IC<7>{}); step(IC<8>{}); step(IC<9>{}); step(IC<10>{}); step(IC<11>{});
    };
    stage(IC<0>{}); stage(IC<1>{}); stage(IC<2>{});
  };
  f32x16 accA, accB;
  pair(IC<0>{}, IC<0>{}, accA, accA);
  pair(IC<1>{}, IC<0>{}, accB, accA);
  pair(IC<2>{}, IC<0>{}, accA, accB);
  pair(IC<3>{}, IC<0>{}, accB, accA);
  pair(IC<4>{}, IC<0>{}, accA, accB);
  pair(IC<5>{}, IC<0>{}, accB, accA);
  STAMP(ws, 0, 5);
  if (NP7) {
    if ((single & 1) != cs) { rdb0 ^= 64; rdb1 ^= 64; rdb2 ^= 64; }
    char* xch = lds + 4 * K0_SLOT + lane * 16;
    char* dst = ws + OFF_ZQ + ((size_t)(t0 / 8 + 4 * tgp + h) * NZQ + FH + single * 32 + (lane & 31)) * 16;
    auto tail = [&](auto CS_) {
      constexpr int CS = CS_.value;
      pair(IC<6>{}, IC<1 + CS>{}, accA, accB);
      f32x4 s0, s1;
#pragma unroll
      for (int j = 0; j < 4; ++j) { s0[j] = accA[8 * (1 - CS) + j]; s1[j] = accA[8 * (1 - CS) + 4 + j]; }
      *(f32x4*)(xch + w * 2048) = s0;
      *(f32x4*)(xch + w * 2048 + 1024) = s1;
      BAR();
      s0 = *(const f32x4*)(xch + (w ^ 4) * 2048);
      s1 = *(const f32x4*)(xch + (w ^ 4) * 2048 + 1024);
      u32x4 o;
      o[0] = pk_bf16(accA[8 * CS + 0] + s0[0], accA[8 * CS + 1] + s0[1]);
      o[1] = pk_bf16(accA[8 * CS + 2] + s0[2], accA[8 * CS + 3] + s0[3]);
      o[2] = pk_bf16(accA[8 * CS + 4] + s1[0], accA[8 * CS + 5] + s1[1]);
      o[3] = pk_bf16(accA[8 * CS + 6] + s1[2], accA[8 * CS + 7] + s1[3]);
      st128<AUX0>(srdO, ws, dst + CS * ustep, o);
    };
    if (cs == 0) tail(IC<0>{}); else tail(IC<1>{});
  } else {
    char* dst = dst0 + 5 * PSTEP;
#pragma unroll
    for (int u = 0; u < 2; ++u) {
      u32x4 o;
#pragma unroll
      for (int j = 0; j < 4; ++j)
        o[j] = GELU ? pk_bf16(gelu_tanh(accB[8 * u + 2 * j]), gelu_tanh(accB[8 * u + 2 * j + 1])) : pk_bf16(accB[8 * u + 2 * j], accB[8 * u + 2 * j + 1]);
      st128<AUX0>(srdO, ws, dst + u * ustep, o);
    }
  }
  WAIT_V(0);
  STAMP(ws, 0, 6);
}

__global__ __launch_bounds__(512, 2) void k0_inproj(const u16* __restrict__ x, const u16* __restrict__ wqkv,
                                                    const u16* __restrict__ w_in, const u16* __restrict__ w_sp,
                                                    const u16* __restrict__ w_out, const u16* __restrict__ wo, char* __restrict__ ws) {
  __shared__ __attribute__((aligned(16))) char lds[6 * K0_SLOT + 8192];
  const int b = blockIdx.x, xcd = b & 7, idx = b >> 3;
  const int cg = xcd * 2 + (idx & 1), rt = idx >> 1;
  if (cg < 6)       k0_body<1, 1, 0>(lds, x, wqkv, w_in, w_sp, w_out, wo, ws, rt, cg);
  else if (cg < 12) k0_body<0, 1, 1>(lds, x, wqkv, w_in, w_sp, w_out, wo, ws, rt, cg);
  else              k0_body<0, 0, 0>(lds, x, wqkv, w_in, w_sp, w_out, wo, ws, rt, cg);
}

constexpr int AT_TG = 320;
constexpr int AT_K = 0, AT_V = 64 * AT_TG, AT_Q = 2 * 64 * AT_TG, AT_ONES = AT_Q + 32 * AT_TG;

constexpr int AT_RED = AT_ONES + 16;
template <int CTRL> DEVI float dpp_mov(float v) {
  return __builtin_bit_cast(float, __builtin_amdgcn_update_dpp(0, __builtin_bit_cast(int, v), CTRL, 0xf, 0xf, true));
}
DEVI float sum32(float a, float b) {
  auto r = __builtin_amdgcn_permlane32_swap(__builtin_bit_cast(u32, a), __builtin_bit_cast(u32, b), false, false);
  u32 r0 = r[0], r1 = r[1];
  return __builtin_bit_cast(float, r0) + __builtin_bit_cast(float, r1);
}
DEVI float sum16(float a, float b) {
  auto r = __builtin_amdgcn_permlane16_swap(__builtin_bit_cast(u32, a), __builtin_bit_cast(u32, b), false, false);
  u32 r0 = r[0], r1 = r[1];
  return __builtin_bit_cast(float, r0) + __builtin_bit_cast(float, r1);
}

__global__ __launch_bounds__(512, 2) void k1_mid(const u16* __restrict__ w_out, const u16* __restrict__ wo, char* __restrict__ ws) {
  __shared__ __attribute__((aligned(16))) char lds[AT_RED + 512];
  const int b = blockIdx.x;
  const int head = (b & 7) * 4 + ((b >> 3) & 3), qb = b >> 5;
  const int tid = threadIdx.x, lane = tid & 63;
  const int w = __builtin_amdgcn_readfirstlane(tid >> 6);
  const int h = lane >> 5;
  STAMP(ws, 1, 0);
  const srd_t srdO = MAKE_SRD(ws);
  const char* zsrc = ws + OFF_ZQ + ((size_t)b * NZQ + tid) * 16;
  u32x4 zv[5];
#pragma unroll
  for (int i = 0; i < 4; ++i) zv[i] = *(const u32x4*)(zsrc + i * 8192);
  zv[4] = u32x4{0u, 0u, 0u, 0u};
  if (w < 4) zv[4] = *(const u32x4*)(zsrc + 4 * 8192);
  const int tl = tid - 256, g = b * 102 + tl;
  const bool tile_on = (tl >= 0) && (tl < 102) && (g < 360 * 72);
  const int fc = g / 72, dq = g - fc * 72;
  u32x4 tr[8];
  if (tile_on) {
    const char* src = (fc < 288) ? (const char*)w_out + (size_t)fc * 8 * 1152 : (const char*)wo + (size_t)(fc - 288) * 8 * 1152;
    src += dq * 16;
#pragma unroll
    for (int i = 0; i < 8; ++i) tr[i] = *(const u32x4*)(src + i * 1152);
  }
  {
    const srd_t srd = MAKE_SRD(ws + OFF_ZQ);
    const u32 l0 = LDS_ADDR(lds);
    const int tgk0 = 32 * qb - 32;
#pragma unroll
    for (int it = 0; it < 7; ++it) {
      const int n = w + 8 * it;
      if (it < 6 || w < 2) {
        const int part = n < 20 ? 1 : n < 40 ? 2 : 0;
        const int nn = n < 20 ? n : n < 40 ? n - 20 : n - 40;
        const int i = nn * 64 + lane;
        const int tg = i / 20;
        int d = i - tg * 20;
        d = d > 17 ? 17 : d;
        int gtg = (part == 0 ? 32 * qb : tgk0) + tg;
        gtg = gtg < 0 ? 0 : gtg;
        const u32 voff = (u32)(gtg * NZQ + d) * 16u;
        const u32 soff = (u32)(FH + 576 * part + 18 * head) * 16u;
        bufl(l0 + (part == 1 ? AT_K : part == 2 ? AT_V : AT_Q) + nn * 1024, voff, srd, soff);
      }
    }
  }
  if (tid == 0) { u32x4 o = {0x3f803f80u, 0x3f803f80u, 0x3f803f80u, 0x3f803f80u}; *(u32x4*)(lds + AT_ONES) = o; }
  f32x2 zx[5][4];
  {
    f32x2 S[4], Q[4];
#pragma unroll
    for (int e = 0; e < 4; ++e) {
#pragma unroll
      for (int i = 0; i < 5; ++i) {
        const u32 u = zv[i][e];
        zx[i][e] = f32x2{bf_lo(u), bf_hi(u)};
        if (i == 0) { S[e] = zx[0][e]; Q[e] = zx[0][e] * zx[0][e]; }
        else { S[e] += zx[i][e]; Q[e] = __builtin_elementwise_fma(zx[i][e], zx[i][e], Q[e]); }
      }
    }
    float r8[8], u4[4];
#pragma unroll
    for (int e = 0; e < 8; ++e) { const float a = S[e >> 1][e & 1], q = Q[e >> 1][e & 1]; r8[e] = sum32(a, q); }
#pragma unroll
    for (int e = 0; e < 4; ++e) {
      float u = sum16(r8[e], r8[e + 4]);
      u += dpp_mov<0x128>(u); u += dpp_mov<0x124>(u); u += dpp_mov<0x122>(u); u += dpp_mov<0x121>(u);
      u4[e] = u;
    }
    if ((lane & 15) == 0) *(f32x4*)(lds + AT_RED + w * 64 + (lane >> 4) * 16) = f32x4{u4[0], u4[1], u4[2], u4[3]};
  }
  WAIT_V(0);
  BAR();
  STAMP(ws, 1, 1);
  {
    const float* red = (const float*)(lds + AT_RED) + (lane & 15);
    float tot = 0.f;
#pragma unroll
    for (int i = 0; i < 8; ++i) tot += red[16 * i];
    const float q = dpp_mov<0x128>(tot);
    const float mean = tot * (1.0f / 2304.0f);
    const float var = fmaxf(__builtin_fmaf(-mean, mean, q * (1.0f / 2304.0f)), 0.f);
    const float rs = __builtin_amdgcn_rsqf(var + 1e-5f), nb = -mean * rs;
    f32x2 rs2[4], nb2[4];
    rs2[0] = f32x2{dpp_mov<0x150>(rs), dpp_mov<0x151>(rs)}; nb2[0] = f32x2{dpp_mov<0x150>(nb), dpp_mov<0x151>(nb)};
    rs2[1] = f32x2{dpp_mov<0x152>(rs), dpp_mov<0x153>(rs)}; nb2[1] = f32x2{dpp_mov<0x152>(nb), dpp_mov<0x153>(nb)};
    rs2[2] = f32x2{dpp_mov<0x154>(rs), dpp_mov<0x155>(rs)}; nb2[2] = f32x2{dpp_mov<0x154>(nb), dpp_mov<0x155>(nb)};
    rs2[3] = f32x2{dpp_mov<0x156>(rs), dpp_mov<0x157>(rs)}; nb2[3] = f32x2{dpp_mov<0x156>(nb), dpp_mov<0x157>(nb)};
    char* dst = ws + OFF_ZN + ((size_t)b * FH + tid) * 16;
#pragma unroll
    for (int i = 0; i < 5; ++i) {
      u32x4 o;
#pragma unroll
      for (int e = 0; e < 4; ++e) {
        const f32x2 y = __builtin_elementwise_fma(zx[i][e], rs2[e], nb2[e]);
        o[e] = pk_bf16(y[0], y[1]);
      }
      if (i < 4 || w < 4) st128<AUX1>(srdO, ws, dst + i * 8192, o);
    }
  }
  if (tile_on) {
    char* dst = ws + OFF_WT + ((size_t)fc * 576 + dq * 8) * 16;
#pragma unroll
    for (int d = 0; d < 8; ++d) {
      u32x4 o;
#pragma unroll
      for (int m = 0; m < 4; ++m)
        o[m] = __builtin_amdgcn_perm(tr[2 * m + 1][d >> 1], tr[2 * m][d >> 1], (d & 1) ? 0x07060302u : 0x05040100u);
      st128<AUX1>(srdO, ws, dst + d * 16, o);
    }
  }
  SB();
  STAMP(ws, 1, 2);
  const int q4 = (lane & 15) >> 2, p4 = lane & 3, g16 = (lane >> 4) & 1;
  u32x4 qf0, qf1;
  {
    const char* qb_ = lds + AT_Q + (4 * w + 2 * g16 + (p4 >> 1)) * AT_TG + (p4 & 1) * 8;
    int d0 = 8 * h + q4;
    qf0 = tr_read2(qb_ + d0 * 16, qb_ + (d0 + 4) * 16);
    int d1 = 16 + q4; d1 = d1 > 17 ? 17 : d1;
    u32x2 t = tr_read(qb_ + d1 * 16);
    qf1[0] = h ? 0u : t[0]; qf1[1] = 0u; qf1[2] = 0u; qf1[3] = 0u;
  }
  const char* kb0 = lds + AT_K + (4 * w + 2 * g16 + (p4 & 1)) * AT_TG + (p4 >> 1) * 8;
  const int kd0 = (8 * h + q4) * 16;
  const int kd1 = (16 + q4 > 17 ? 17 : 16 + q4) * 16;
  const int n = lane & 31;
  const float NEG = -3.0e38f;
  f32x16 s[9];
#pragma unroll
  for (int jj = 0; jj < 9; ++jj) {
    const char* kb = kb0 + jj * 4 * AT_TG;
    u32x4 k0 = tr_read2(kb + kd0, kb + kd0 + 64);
    u32x2 k1a = tr_read(kb + kd1);
    u32x4 k1 = {k1a[0], k1a[1], k1a[0], k1a[1]};
    f32x16 z;
#pragma unroll
    for (int i = 0; i < 16; ++i) z[i] = 0.f;
    s[jj] = mfma32(k1, qf1, mfma32(k0, qf0, z));
  }
  if (qb == 0) {
#pragma unroll
    for (int jj = 0; jj < 8; ++jj)
      if (jj < 8 - w) {
#pragma unroll
        for (int r = 0; r < 16; ++r) s[jj][r] = NEG;
      }
  }
  u32 mk[8];
  {
    const int nb = n - 8 * h;
#pragma unroll
    for (int j = 0; j < 8; ++j) {
      const int k0 = ((2 * j) & 7) + 16 * (j >> 2);
      const bool c0 = k0 <= nb, c1 = k0 + 1 <= nb;
      s[0][2 * j] = c0 ? s[8][2 * j] : s[0][2 * j];
      s[0][2 * j + 1] = c1 ? s[8][2 * j + 1] : s[0][2 * j + 1];
      mk[j] = c1 ? 0xffffffffu : (c0 ? 0x0000ffffu : 0u);
    }
  }
  float m = __builtin_fmaxf(s[0][0], s[0][1]);
#pragma unroll
  for (int jj = 0; jj < 8; ++jj)
#pragma unroll
    for (int r = (jj == 0 ? 2 : 0); r < 16; r += 2) m = __builtin_fmaxf(__builtin_fmaxf(m, s[jj][r]), s[jj][r + 1]);
  {
    auto r = __builtin_amdgcn_permlane32_swap(__builtin_bit_cast(u32, m), __builtin_bit_cast(u32, m), false, false);
    u32 r0 = r[0], r1 = r[1];
    m = __builtin_fmaxf(__builtin_bit_cast(float, r0), __builtin_bit_cast(float, r1));
  }
  STAMP(ws, 1, 3);
  const float c = 0.23570226039551584f * 1.4426950408889634f;
  const f32x2 c2 = {c, c}, mc2 = {-m * c, -m * c};
  f32x16 o;
#pragma unroll
  for (int i = 0; i < 16; ++i) o[i] = 0.f;
  const char* vb0 = (n == 18) ? lds + AT_ONES : lds + AT_V + (4 * w + h) * AT_TG + n * 16;
  const int vstep = (n == 18) ? 0 : AT_TG;
  u32x4 va = *(const u32x4*)(vb0 + 4 * vstep), vb = *(const u32x4*)(vb0 + 6 * vstep);
#pragma unroll
  for (int jj = 1; jj <= 8; ++jj) {
    const int tt = jj & 7;
    const int nx = jj < 8 ? jj + 1 : 0;
    const u32x4 van = *(const u32x4*)(vb0 + (4 * nx) * vstep), vbn = *(const u32x4*)(vb0 + (4 * nx + 2) * vstep);
    u32x4 p0, p1;
#pragma unroll
    for (int j = 0; j < 4; ++j) {
      const f32x2 a = __builtin_elementwise_fma(f32x2{s[tt][2 * j], s[tt][2 * j + 1]}, c2, mc2);
      const f32x2 bq = __builtin_elementwise_fma(f32x2{s[tt][8 + 2 * j], s[tt][8 + 2 * j + 1]}, c2, mc2);
      p0[j] = pk_bf16(fexp2(a[0]), fexp2(a[1]));
      p1[j] = pk_bf16(fexp2(bq[0]), fexp2(bq[1]));
    }
    if (jj < 8) {
      o = mfma32(va, p0, o);
      o = mfma32(vb, p1, o);
    } else {
      u32x4 m0, m1;
#pragma unroll
      for (int j = 0; j < 4; ++j) { m0[j] = p0[j] & mk[j]; m1[j] = p1[j] & mk[4 + j]; }
      o = mfma32(va, m0, o);
      o = mfma32(vb, m1, o);
#pragma unroll
      for (int j = 0; j < 4; ++j) { m0[j] ^= p0[j]; m1[j] ^= p1[j]; }
      o = mfma32(van, m0, o);
      o = mfma32(vbn, m1, o);
    }
    va = van; vb = vbn;
    SB();
  }
  STAMP(ws, 1, 4);
  float l = o[10];
  {
    auto r = __builtin_amdgcn_permlane32_swap(__builtin_bit_cast(u32, l), __builtin_bit_cast(u32, l), false, false);
    u32 r0 = r[0];
    l = __builtin_bit_cast(float, r0);
  }
  const float inv = 1.0f / l;
  const int t = 256 * qb + 32 * w + n;
  char* po = ws + OFF_PO + (size_t)t * 16;
  const int cb = 18 * head + 4 * h;
#pragma unroll
  for (int pr = 0; pr < 5; ++pr) {
    const int r = 2 * pr;
    const int d = (r & 3) + 8 * (r >> 2);
    if (pr < 4 || h == 0) {
      int cc = cb + d;
      u32 v = pk_bf16(o[r] * inv, o[r + 1] * inv);
      st32<AUX1>(srdO, ws, po + (size_t)(288 + (cc >> 3)) * 2048 * 16 + (cc & 7) * 2, v);
    }
  }
  STAMP(ws, 1, 5);
}

constexpr int K2_SLOT = 16384 + 8 * 2304;

template <int EXTRA>
DEVI void k2_body(char* lds, const u16* __restrict__ wsp, const u16* __restrict__ bsp, char* __restrict__ ws) {
  const int tid = threadIdx.x, lane = tid & 63;
  const int w = __builtin_amdgcn_readfirstlane(tid >> 6);
  const int kh = w >> 2, tq = w & 3;
  const int b = blockIdx.x, xcd = b & 7, idx = b >> 3;
  const int tb = (xcd & 3) * 4 + (idx & 3), fb = (xcd >> 2) * 8 + (idx >> 2);
  const int t0 = tb * 128, f0 = fb * 144;
  STAMP(ws, 2, 0);

  u32 wv0, wv1, zv[3];
  {
    int r0 = 16 * w + (lane >> 3), r1 = r0 + 8;
    wv0 = (u32)(r0 * 4096 + (((lane & 7) ^ ((r0 >> 1) & 7)) * 16));
    wv1 = (u32)(r1 * 4096 + (((lane & 7) ^ ((r1 >> 1) & 7)) * 16));
#pragma unroll
    for (int j = 0; j < 3; ++j) {
      int e = (w + 8 * j) * 64 + lane;
      int tg = e / 144, f = e - tg * 144;
      zv[j] = (u32)((tg * FH + f) * 16);
    }
  }
  const srd_t wsrd = MAKE_SRD((const char*)wsp + (size_t)t0 * 4096);
  const srd_t zsrd = MAKE_SRD(ws + OFF_ZN + (size_t)f0 * 16);
  const u32 ldsb = LDS_ADDR(lds) + w * 1024;
  auto piece = [&](int it, int j) {
    const u32 sl = ldsb + (it & 3) * K2_SLOT;
    const u32 wo = it * 128, zo = it * (8 * FH * 16);
    if (j == 0) BUFL(sl + w * 1024, wv0, wsrd, wo);
    if (j == 1) BUFL(sl + 16384, zv[0], zsrd, zo);
    if (j == 2) BUFL(sl + w * 1024 + 1024, wv1, wsrd, wo);
    if (j == 3) BUFL(sl + 16384 + 8192, zv[1], zsrd, zo);
    if (j == 4 && EXTRA) BUFL(sl + 16384 + 16384, zv[2], zsrd, zo);
  };
  auto issue = [&](int it) { piece(it, 0); piece(it, 1); piece(it, 2); piece(it, 3); piece(it, 4); };
#define K2_WAIT(n) do { if (EXTRA) WAIT_V_(n * 5); else WAIT_V_(n * 4); } while (0)
#define K2_WAITZ(n) do { if (EXTRA) WAIT_V_(n * 5 + 12); else WAIT_V_(n * 4 + 12); } while (0)
  issue(0); issue(1); issue(2); issue(3);

  const int g = lane >> 4, l15 = lane & 15;
  f32x4 acc[9][2];
#pragma unroll
  for (int i = 0; i < 9; ++i) { acc[i][0] = f32x4{0.f, 0.f, 0.f, 0.f}; acc[i][1] = f32x4{0.f, 0.f, 0.f, 0.f}; }
  int woff[2];
#pragma unroll
  for (int ti = 0; ti < 2; ++ti) {
    int row = 32 * tq + 16 * ti + l15;
    woff[ti] = row * 128 + (((4 * kh + g) ^ ((row >> 1) & 7)) * 16);
  }
  const int zoff = 16384 + (4 * kh + g) * 2304 + l15 * 16;

  struct Frags { u32x4 a[9]; u32x4 b[2]; };
  auto rdb = [&](int it, Frags& F) {
    const char* sl = lds + (it & 3) * K2_SLOT;
    F.b[0] = *(const u32x4*)(sl + woff[0]);
    F.b[1] = *(const u32x4*)(sl + woff[1]);
  };
  auto rda = [&](int it, Frags& F, int fi) {
    const char* sl = lds + (it & 3) * K2_SLOT;
    F.a[fi] = *(const u32x4*)(sl + zoff + fi * 256);
  };
  auto mm1 = [&](const Frags& F, int fi) {
    acc[fi][0] = mfma16(F.a[fi], F.b[0], acc[fi][0]);
    acc[fi][1] = mfma16(F.a[fi], F.b[1], acc[fi][1]);
  };
  auto stage = [&](const Frags& C, Frags& N, int rit, int lit, auto LDc, auto RDc) {
    constexpr bool LD = decltype(LDc)::value, RD = decltype(RDc)::value;
    SB();
    if (RD) rdb(rit, N);
    mm1(C, 0); SB();
    if (RD) { rda(rit, N, 0); rda(rit, N, 1); }
    mm1(C, 1); if (LD) piece(lit, 0); SB();
    if (RD) { rda(rit, N, 2); rda(rit, N, 3); }
    mm1(C, 2); SB();
    if (RD) { rda(rit, N, 4); rda(rit, N, 5); }
    mm1(C, 3); if (LD) piece(lit, 1); SB();
    if (RD) { rda(rit, N, 6); rda(rit, N, 7); }
    mm1(C, 4); SB();
    if (RD) rda(rit, N, 8);
    mm1(C, 5); if (LD) piece(lit, 2); SB();
    mm1(C, 6); SB();
    mm1(C, 7); if (LD) piece(lit, 3); SB();
    mm1(C, 8); if (LD) piece(lit, 4); SB();
  };
  using LDT = std::true_type;
  using T = std::true_type; using Fa = std::false_type;
  Frags F0, F1;
  K2_WAIT(3); BAR();
  STAMP(ws, 2, 1);
  rdb(0, F0);
#pragma unroll
  for (int fi = 0; fi < 9; ++fi) rda(0, F0, fi);
  for (int it = 0; it < 28; it += 2) {
    K2_WAIT(2); BAR();
    stage(F0, F1, it + 1, it + 4, LDT{}, T{});
    K2_WAIT(2); BAR();
    stage(F1, F0, it + 2, it + 5, LDT{}, T{});
  }
  STAMP(ws, 2, 2);
  const size_t eoff = ((size_t)(f0 / 8 + (g >> 1)) * 2048 + t0 + 32 * tq + l15) * 16 + (g & 1) * 8;
  u32x2 z1v[5][2];
  u32 biasr[2];
  K2_WAIT(2); BAR();
#pragma unroll
  for (int fi = 0; fi < 5; ++fi) {
    const int fa = (kh == 0) ? fi : (fi < 4 ? 5 + fi : 8);
#pragma unroll
    for (int ti = 0; ti < 2; ++ti)
      z1v[fi][ti] = *(const u32x2*)(ws + OFF_Z1 + eoff + ((size_t)fa * 2 * 2048 + ti * 16) * 16);
  }
#pragma unroll
  for (int ti = 0; ti < 2; ++ti) biasr[ti] = (u32)bsp[t0 + 32 * tq + 16 * ti + l15];
  stage(F0, F1, 29, 0, Fa{}, T{});
  K2_WAITZ(1); BAR(); stage(F1, F0, 30, 0, Fa{}, T{});
  K2_WAITZ(0); BAR(); stage(F0, F1, 31, 0, Fa{}, T{});
  stage(F1, F0, 0, 0, Fa{}, Fa{});
  STAMP(ws, 2, 3);
  float bias[2] = {bf_lo(biasr[0]), bf_lo(biasr[1])};
  BAR();
  char* xch = lds + tq * 18432 + lane * 16;
  auto finish = [&](auto KH) {
    constexpr int khc = decltype(KH)::value;
    constexpr int lo = khc ? 5 : 0, hi = khc ? 9 : 5;
#pragma unroll
    for (int fi = 0; fi < 9; ++fi)
      if (fi < lo || fi >= hi) {
        *(f32x4*)(xch + (fi * 2 + 0) * 1024) = acc[fi][0];
        *(f32x4*)(xch + (fi * 2 + 1) * 1024) = acc[fi][1];
      }
    BAR();
#pragma unroll
    for (int fi = lo; fi < hi; ++fi)
#pragma unroll
      for (int ti = 0; ti < 2; ++ti) {
        f32x4 o = *(const f32x4*)(xch + (fi * 2 + ti) * 1024);
        f32x4 gq = acc[fi][ti] + o;
        u32x2 z = z1v[fi - lo][ti];
        u32x2 r;
        r[0] = pk_bf16(bf_lo(z[0]) * (gq[0] + bias[ti]), bf_hi(z[0]) * (gq[1] + bias[ti]));
        r[1] = pk_bf16(bf_lo(z[1]) * (gq[2] + bias[ti]), bf_hi(z[1]) * (gq[3] + bias[ti]));
        st64<AUX2>(MAKE_SRD(ws), ws, ws + OFF_PO + eoff + ((size_t)fi * 2 * 2048 + ti * 16) * 16, r);
      }
  };
  if (kh == 0) finish(std::integral_constant<int, 0>{});
  else         finish(std::integral_constant<int, 1>{});
  STAMP(ws, 2, 4);
}

__global__ __launch_bounds__(512, 2) void k2_spatial(const u16* __restrict__ wsp, const u16* __restrict__ bsp, char* __restrict__ ws) {
  __shared__ __attribute__((aligned(16))) char lds[4 * K2_SLOT];
  if (__builtin_amdgcn_readfirstlane(threadIdx.x >> 6) < 2) k2_body<1>(lds, wsp, bsp, ws);
  else                   k2_body<0>(lds, wsp, bsp, ws);
}

constexpr int K3_SLOT = 8192 + 9216;

template <int EXTRA>
DEVI void k3_body(char* lds, const u16* __restrict__ x, char* __restrict__ ws, u16* __restrict__ out) {
  const int tid = threadIdx.x, lane = tid & 63;
  const int w = __builtin_amdgcn_readfirstlane(tid >> 6);
  const int kh = w >> 2, th = (w >> 1) & 1, dh = (w & 1) ^ kh;
  const int b = blockIdx.x, xcd = b & 7, idx = b >> 3;
  const int tb = xcd * 4 + (idx & 3), db = idx >> 2;
  const int t0 = tb * 64, d0 = db * 72;
  STAMP(ws, 3, 0);

  u32 pv = (u32)((w * 2048 + lane) * 16), wv[2];
#pragma unroll
  for (int j = 0; j < 2; ++j) {
    int e = (w + 8 * j) * 64 + lane;
    int fc = e / 72, dp = e - fc * 72;
    int d = ((fc & 1) && dp < 64) ? (dp ^ 8) : dp;
    wv[j] = (u32)((fc * 576 + d) * 16);
  }
  const srd_t psrd = MAKE_SRD(ws + OFF_PO + (size_t)t0 * 16);
  const srd_t wsrd = MAKE_SRD(ws + OFF_WT + (size_t)d0 * 16);
  const u32 ldsb = LDS_ADDR(lds) + w * 1024;
  auto piece = [&](int it, int j) {
    const u32 sl = ldsb + (it & 7) * K3_SLOT;
    if (j == 0) BUFL(sl, pv, psrd, it * (8 * 2048 * 16));
    if (j == 1) BUFL(sl + 8192, wv[0], wsrd, it * (8 * 576 * 16));
    if (j == 2 && EXTRA) BUFL(sl + 8192 + 8192, wv[1], wsrd, it * (8 * 576 * 16));
  };
  auto issue = [&](int it) { piece(it, 0); piece(it, 1); piece(it, 2); };
#define K3_WAIT(n, z) do { if (EXTRA) WAIT_V_((n) * 3 + (z)); else WAIT_V_((n) * 2 + (z)); } while (0)
#pragma unroll
  for (int i = 0; i < 8; ++i) issue(i);

  const int g = lane >> 4, l15 = lane & 15;
  const int poff = (4 * kh + g) * 1024 + (32 * th + l15) * 16;
  int aoff[3];
#pragma unroll
  for (int di = 0; di < 3; ++di) {
    int d = 48 * dh + 16 * di + l15;
    if (d >= 72) d = 64 + (l15 & 7);
    int dp = ((g & 1) && d < 64) ? (d ^ 8) : d;
    aoff[di] = 8192 + ((4 * kh + g) * 72 + dp) * 16;
  }
  f32x4 accB[3][2], accS[3][2];
#pragma unroll
  for (int i = 0; i < 3; ++i)
#pragma unroll
    for (int j = 0; j < 2; ++j) { accB[i][j] = f32x4{0.f, 0.f, 0.f, 0.f}; accS[i][j] = f32x4{0.f, 0.f, 0.f, 0.f}; }

  struct Frags { u32x4 a[3]; u32x4 b[2]; };
  auto body = [&](auto DHc) {
    constexpr int DH = decltype(DHc)::value;
    constexpr int NA = DH ? 2 : 3;
    auto rdb = [&](int it, Frags& F) {
      const char* sl = lds + (it & 7) * K3_SLOT;
      F.b[0] = *(const u32x4*)(sl + poff);
      F.b[1] = *(const u32x4*)(sl + poff + 256);
    };
    auto rda = [&](int it, Frags& F) {
      const char* sl = lds + (it & 7) * K3_SLOT;
#pragma unroll
      for (int di = 0; di < NA; ++di) F.a[di] = *(const u32x4*)(sl + aoff[di]);
    };
    auto mm1 = [&](const Frags& F, f32x4 (&acc)[3][2], int di) {
      acc[di][0] = mfma16(F.a[di], F.b[0], acc[di][0]);
      acc[di][1] = mfma16(F.a[di], F.b[1], acc[di][1]);
    };
    auto stage = [&](const Frags& C, Frags& N, f32x4 (&acc)[3][2], int rit, int lit, auto LDc, auto RDc) {
      constexpr bool LD = decltype(LDc)::value, RD = decltype(RDc)::value;
      SB();
      if (RD) rdb(rit, N);
      mm1(C, acc, 0); if (LD) piece(lit, 0); SB();
      if (RD) rda(rit, N);
      mm1(C, acc, 1); if (LD) piece(lit, 1); SB();
      if (NA == 3) mm1(C, acc, 2);
      if (LD) piece(lit, 2);
      SB();
    };
    using LDT = std::true_type;
    using T = std::true_type; using Fa = std::false_type;
    Frags F0, F1;
    K3_WAIT(7, 0); BAR();
    STAMP(ws, 3, 1);
    rdb(0, F0); rda(0, F0);
    for (int it = 0; it < 36; it += 2) {
      K3_WAIT(6, 0); BAR();
      stage(F0, F1, accB, it + 1, it + 8, LDT{}, T{});
      K3_WAIT(6, 0); BAR();
      stage(F1, F0, accB, it + 2, it + 9, LDT{}, T{});
    }
    STAMP(ws, 3, 2);
    K3_WAIT(6, 0); BAR();
    stage(F0, F1, accS, 37, 44, LDT{}, T{});
    const int t = t0 + 32 * th + 16 * kh + l15;
    u32x2 xv[3];
#pragma unroll
    for (int di = 0; di < 3; ++di) {
      int d = 48 * dh + 16 * di + 4 * g;
      d = d < 72 ? d : 0;
      xv[di] = *(const u32x2*)(x + (size_t)t * 576 + d0 + d);
    }
    K3_WAIT(6, 3); BAR(); stage(F1, F0, accS, 38, 0, Fa{}, T{});
    K3_WAIT(5, 3); BAR(); stage(F0, F1, accS, 39, 0, Fa{}, T{});
    K3_WAIT(4, 3); BAR(); stage(F1, F0, accS, 40, 0, Fa{}, T{});
    K3_WAIT(3, 3); BAR(); stage(F0, F1, accS, 41, 0, Fa{}, T{});
    K3_WAIT(2, 3); BAR(); stage(F1, F0, accS, 42, 0, Fa{}, T{});
    K3_WAIT(1, 3); BAR(); stage(F0, F1, accS, 43, 0, Fa{}, T{});
    K3_WAIT(0, 3); BAR(); stage(F1, F0, accS, 44, 0, Fa{}, T{});
    stage(F0, F1, accS, 0, 0, Fa{}, Fa{});
    asm volatile("" ::"v"(xv[0]), "v"(xv[1]), "v"(xv[2]));
    STAMP(ws, 3, 3);
    BAR();
    char* xch = lds + ((th * 2 + dh) * 2) * 6144 + lane * 16;
    auto finish = [&](auto KH) {
      constexpr int own = decltype(KH)::value, oth = own ^ 1;
#pragma unroll
      for (int di = 0; di < NA; ++di) {
        *(f32x4*)(xch + oth * 6144 + di * 1024) = accB[di][oth];
        *(f32x4*)(xch + oth * 6144 + 3072 + di * 1024) = accS[di][oth];
      }
      BAR();
#pragma unroll
      for (int di = 0; di < NA; ++di) {
        f32x4 Bv = accB[di][own] + *(const f32x4*)(xch + own * 6144 + di * 1024);
        f32x4 Sv = accS[di][own] + *(const f32x4*)(xch + own * 6144 + 3072 + di * 1024);
        int d = 48 * dh + 16 * di + 4 * g;
        if (d < 72) {
          u32x2 r;
          r[0] = pk_bf16(bf_lo(xv[di][0]) + Sv[0] * silu_f(Bv[0]), bf_hi(xv[di][0]) + Sv[1] * silu_f(Bv[1]));
          r[1] = pk_bf16(bf_lo(xv[di][1]) + Sv[2] * silu_f(Bv[2]), bf_hi(xv[di][1]) + Sv[3] * silu_f(Bv[3]));
          st64<AUX3>(MAKE_SRD(out), (const char*)out, (const char*)(out + (size_t)t * 576 + d0 + d), r);
        }
      }
    };
    if (kh == 0) finish(std::integral_constant<int, 0>{});
    else         finish(std::integral_constant<int, 1>{});
    STAMP(ws, 3, 4);
  };
  if (dh == 0) body(std::integral_constant<int, 0>{});
  else         body(std::integral_constant<int, 1>{});
}

__global__ __launch_bounds__(512, 2) void k3_out(const u16* __restrict__ x, char* __restrict__ ws, u16* __restrict__ out) {
  __shared__ __attribute__((aligned(16))) char lds[8 * K3_SLOT];
  if (__builtin_amdgcn_readfirstlane(threadIdx.x >> 6) == 0) k3_body<1>(lds, x, ws, out);
  else                                                       k3_body<0>(lds, x, ws, out);
}

__global__ void k_empty(char* ws) { if (ws == nullptr) ws[0] = 1; }
static inline void launch_stage(int st, void* const* d_in, void* d_out, void* d_ws, hipStream_t stream) {
  const u16* x = (const u16*)d_in[0];
  const u16* wqkv = (const u16*)d_in[1];
  const u16* wo = (const u16*)d_in[2];
  const u16* w_in = (const u16*)d_in[4];
  const u16* w_sp = (const u16*)d_in[5];
  const u16* b_sp = (const u16*)d_in[6];
  const u16* w_out = (const u16*)d_in[7];
  char* ws = (char*)d_ws;
  if (st == 0) k0_inproj<<<256, 512, 0, stream>>>(x, wqkv, w_in, w_sp, w_out, wo, ws);
  if (st == 1) k1_mid<<<256, 512, 0, stream>>>(w_out, wo, ws);
  if (st == 2) k2_spatial<<<256, 512, 0, stream>>>(w_sp, b_sp, ws);
  if (st == 4) k_empty<<<256, 512, 0, stream>>>(ws);
  if (st == 3) k3_out<<<256, 512, 0, stream>>>(x, ws, (u16*)d_out);
}

extern "C" void kernel_launch(void* const* d_in, const int* in_sizes, int n_in, void* d_out, int out_size, void* d_ws,
                              size_t ws_size, hipStream_t stream) {
  (void)in_sizes; (void)n_in; (void)out_size; (void)ws_size;
  for (int st = 0; st < 4; ++st) launch_stage(st, d_in, d_out, d_ws, stream);
}
```

```cpp
#include <hip/hip_runtime.h>
#include <hip/hip_bf16.h>
#include <stdint.h>
#include <type_traits>

typedef unsigned int u32;
typedef unsigned short u16;
typedef __attribute__((ext_vector_type(8))) __bf16 bf16x8;
typedef __attribute__((ext_vector_type(2))) __bf16 bf16x2;
typedef __attribute__((ext_vector_type(4))) short s16x4;
typedef __attribute__((ext_vector_type(2))) float f32x2;
typedef __attribute__((ext_vector_type(4))) float f32x4;
typedef __attribute__((ext_vector_type(16))) float f32x16;
typedef __attribute__((ext_vector_type(2))) u32 u32x2;
typedef __attribute__((ext_vector_type(4))) u32 u32x4;

#define DEVI __device__ __forceinline__
#define AS1 __attribute__((address_space(1)))
#define AS3 __attribute__((address_space(3)))
#define GLDS(g, l) __builtin_amdgcn_global_load_lds((const AS1 void*)(g), (AS3 void*)(l), 16, 0, 0)
#define WAIT_V(n) asm volatile("s_waitcnt vmcnt(" #n ")" ::: "memory")
#define WAIT_V_(n) asm volatile("s_waitcnt vmcnt(%0)" ::"n"(n) : "memory")
#define SB() __builtin_amdgcn_sched_barrier(0)
typedef __amdgpu_buffer_rsrc_t srd_t;
#define BUFL(m0v, voff, srd, soff) \
  asm volatile("s_mov_b32 m0, %0\n\ts_nop 0\n\tbuffer_load_dwordx4 %1, %2, %3 offen lds" ::"s"(m0v), "v"(voff), "s"(srd), "s"(soff) : "m0", "memory")
#define MAKE_SRD(p) __builtin_amdgcn_make_buffer_rsrc((void*)(p), (short)0, 0x7fffffff, 0x00020000)
#define LDS_ADDR(p) ((u32)(uintptr_t)(AS3 char*)(p))
#define WAIT_L0() asm volatile("s_waitcnt lgkmcnt(0)" ::: "memory")
#define SGB(m, n) __builtin_amdgcn_sched_group_barrier(m, n, 0)
#define BAR() do { WAIT_L0(); __builtin_amdgcn_s_barrier(); asm volatile("" ::: "memory"); } while (0)

#define AUX0 16
#define AUX1 16
#define AUXT 16
#define TILE_POS 1
#define TILE_LDS 1
#define AUXO 0
#define AUX2 16
#define AUX3 0
template <int AUX> DEVI void st128(srd_t r, const char* base, const char* p, u32x4 v) { __builtin_amdgcn_raw_buffer_store_b128(v, r, (u32)(p - base), 0, AUX); }
template <int AUX> DEVI void st64(srd_t r, const char* base, const char* p, u32x2 v) { __builtin_amdgcn_raw_buffer_store_b64(v, r, (u32)(p - base), 0, AUX); }
template <int AUX> DEVI void st32(srd_t r, const char* base, const char* p, u32 v) { __builtin_amdgcn_raw_buffer_store_b32(v, r, (u32)(p - base), 0, AUX); }

constexpr int S_ = 2048, D_ = 576, FH = 2304, NZQ = 4032;
constexpr size_t OFF_Z1 = 0;
constexpr size_t OFF_ZQ = OFF_Z1 + (size_t)288 * 2048 * 16;
constexpr size_t OFF_ZN = OFF_ZQ + (size_t)256 * NZQ * 16;
constexpr size_t OFF_PO = OFF_ZN + (size_t)256 * FH * 16;
constexpr size_t OFF_WT = OFF_PO + (size_t)360 * 2048 * 16;
constexpr size_t WS_END = OFF_WT + (size_t)360 * 576 * 16;

#define STAMP(ws, k, i)
DEVI float bf_lo(u32 v) { return __builtin_bit_cast(float, v << 16); }
DEVI float bf_hi(u32 v) { return __builtin_bit_cast(float, v & 0xffff0000u); }
DEVI u32 pk_bf16(float a, float b) {
  f32x2 v = {a, b};
  return __builtin_bit_cast(u32, __builtin_convertvector(v, bf16x2));
}
DEVI f32x4 mfma16(u32x4 a, u32x4 b, f32x4 c) {
  return __builtin_amdgcn_mfma_f32_16x16x32_bf16(__builtin_bit_cast(bf16x8, a), __builtin_bit_cast(bf16x8, b), c, 0, 0, 0);
}
DEVI f32x16 mfma32(u32x4 a, u32x4 b, f32x16 c) {
  return __builtin_amdgcn_mfma_f32_32x32x16_bf16(__builtin_bit_cast(bf16x8, a), __builtin_bit_cast(bf16x8, b), c, 0, 0, 0);
}
DEVI u32x2 tr_read(const char* p) {
  s16x4 v = __builtin_amdgcn_ds_read_tr16_b64_v4i16((AS3 s16x4*)p);
  return __builtin_bit_cast(u32x2, v);
}
DEVI u32x4 tr_read2(const char* p0, const char* p1) {
  u32x2 a = tr_read(p0), b = tr_read(p1);
  u32x4 r = {a[0], a[1], b[0], b[1]};
  return r;
}
DEVI float fexp2(float x) { return __builtin_amdgcn_exp2f(x); }
DEVI float frcp(float x) { return __builtin_amdgcn_rcpf(x); }
DEVI float gelu_tanh(float v) {
  const float c0 = -2.0f * 0.7978845608028654f * 1.4426950408889634f;
  const float c1 = c0 * 0.044715f;
  float a = __builtin_fmaf(v * v, c1, c0);
  float e = fexp2(v * a);
  return v * frcp(1.0f + e);
}
DEVI float silu_f(float v) { return v * frcp(1.0f + fexp2(v * -1.4426950408889634f)); }
DEVI int swap23(int i) { return (i & ~12) | ((i & 4) << 1) | ((i & 8) >> 1); }

#define K0_G0 1
constexpr int K0_SLOT = 24576;
#define MAKE_SRDN(p, n) __builtin_amdgcn_make_buffer_rsrc((void*)(p), (short)0, (int)(n), 0x00020000)
template <int V> using IC = std::integral_constant<int, V>;
DEVI void bufl(u32 m0v, u32 voff, srd_t srd, u32 soff) { BUFL(m0v, voff, srd, soff); }
#define CODE_PF 0
#define CODE_WARM(SYM, NB, LEN, LDSDUMP)                                                                             \
  do {                                                                                                           \
    if (CODE_PF && (blockIdx.x >> 3) < (NB) && __builtin_amdgcn_readfirstlane(threadIdx.x >> 6) == 7) {          \
      unsigned long long pc_; int off_;                                                                          \
      asm volatile("s_getpc_b64 %0\n.Lcw%=:\n\ts_mov_b32 %1, " SYM "-.Lcw%=" : "=s"(pc_), "=s"(off_));           \
      const char* code_ = (const char*)(pc_ + (long long)off_);                                                  \
      bufl((LDSDUMP), (blockIdx.x >> 3) * 8192u + (threadIdx.x & 63) * 128u, MAKE_SRDN(code_, (LEN)), 0u);      \
    }                                                                                                            \
  } while (0)
DEVI float dpp_xor1(float v) { return __builtin_bit_cast(float, __builtin_amdgcn_update_dpp(0, __builtin_bit_cast(int, v), 0xB1, 0xf, 0xf, true)); }
DEVI float dpp_xor2(float v) { return __builtin_bit_cast(float, __builtin_amdgcn_update_dpp(0, __builtin_bit_cast(int, v), 0x4E, 0xf, 0xf, true)); }

template <int ZORI, int GELU, int NP7>
DEVI void k0_body(char* lds, const u16* __restrict__ x, const u16* __restrict__ wqkv, const u16* __restrict__ w_in,
                  const u16* __restrict__ w_sp, const u16* __restrict__ w_out, const u16* __restrict__ wo,
                  char* __restrict__ ws, int rt, int cg) {
  const int tid = threadIdx.x, lane = tid & 63;
  const int w = __builtin_amdgcn_readfirstlane(tid >> 6);
  const int tgp = w & 3, cs = w >> 2;
  const int h = lane >> 5;
  const int t0 = rt * 128;
  const int tok = swap23(lane & 31);
  constexpr int NPAIR = 6 + NP7;
  constexpr bool IS_Q = !GELU;
  STAMP(ws, 0, 0);

  u32x4 xr[18];
  const int xrow = 16 * w + (lane >> 2), xj = lane & 3;
  {
    const char* xp = (const char*)x + (size_t)(t0 + xrow) * 1152 + xj * 16;
#pragma unroll
    for (int i = 0; i < 18; ++i) xr[i] = *(const u32x4*)(xp + i * 64);
  }
  constexpr int WSTR = IS_Q ? 3456 : 9216;
  const int wrow = 24 * w + (lane >> 3);
  const int wch = (lane & 7) ^ (((lane >> 4) & 1) << 2);
  const srd_t srdW = MAKE_SRD(IS_Q ? (const char*)wqkv + (cg - 12) * 768 : (const char*)w_in + cg * 768);
  const u32 voffW = wrow * WSTR + wch * 16;
  const int single = 48 + (cg - 6);
  const srd_t srdS = MAKE_SRD((const char*)wqkv + (NP7 ? (single >> 1) * 128 : 0));
  const u32 voffS = wrow * 3456 + wch * 16;
  const u32 ldsw = LDS_ADDR(lds) + w * 3072;
  const srd_t srdP0 = MAKE_SRD((const char*)w_sp + (size_t)blockIdx.x * 32768);
  const srd_t srdZ = MAKE_SRDN(x, 0);
  const srd_t srdO = MAKE_SRD(ws);
  const u32 pidx = blockIdx.x * 8 + w;
  const u32 vl16 = lane * 16;
  auto issue = [&](auto PQ_, auto TH_, auto SL_, auto N_) {
    constexpr int pq = PQ_.value, TH = TH_.value, SL = SL_.value, N = N_.value;
    const u32 m0v = ldsw + SL * K0_SLOT + N * 1024;
    if constexpr (pq < 6) {
      bufl(m0v, voffW, srdW, (u32)(pq * 128 + (TH * 192 + 8 * N) * WSTR));
    } else if constexpr (NP7 && pq == 6) {
      bufl(m0v, voffS, srdS, (u32)((TH * 192 + 8 * N) * 3456));
    } else {
      bufl(m0v, vl16, srdZ, 0u);
    }
  };
  auto pf = [&](auto I_) {
    constexpr int i = I_.value;
    const u32 m0v = LDS_ADDR(lds) + 6 * K0_SLOT + w * 1024;
    if constexpr (i < 4) {
      bufl(m0v, vl16, srdP0, (w * 4 + i) * 1024);
    } else if constexpr (i < 6) {
      const u32 c = pidx * 2 + (i - 4);
      const bool ok = c < 2592;
      bufl(m0v, vl16, MAKE_SRDN(w_out, ok ? FH * D_ * 2 : 0), ok ? c * 1024 : 0u);
    } else if constexpr (i == 6) {
      const bool ok = pidx < 648;
      bufl(m0v, vl16, MAKE_SRDN(wo, ok ? D_ * D_ * 2 : 0), ok ? pidx * 1024 : 0u);
    }
  };
  auto issue3 = [&](auto pq, auto TH_, auto SL_) { issue(pq, TH_, SL_, IC<0>{}); issue(pq, TH_, SL_, IC<1>{}); issue(pq, TH_, SL_, IC<2>{}); };

  u32x4 xa[36];
  {
    float s1 = 0.f, s2 = 0.f;
    const bf16x2 ones = __builtin_bit_cast(bf16x2, 0x3f803f80u);
#pragma unroll
    for (int i = 0; i < 18; ++i) {
#pragma unroll
      for (int j = 0; j < 4; ++j) {
        const u32 xv = xr[i][j];
        bf16x2 v = __builtin_bit_cast(bf16x2, xv);
        s1 = __builtin_amdgcn_fdot2_f32_bf16(v, ones, s1, false);
        s2 = __builtin_amdgcn_fdot2_f32_bf16(v, v, s2, false);
      }
    }
    s1 += dpp_xor1(s1); s2 += dpp_xor1(s2);
    s1 += dpp_xor2(s1); s2 += dpp_xor2(s2);
    const float mean = s1 * (1.0f / 576.0f);
    const float var = fmaxf(s2 * (1.0f / 576.0f) - mean * mean, 0.f);
    const float rstd = __builtin_amdgcn_rsqf(var + 1e-5f);
    const float nb = -mean * rstd;
    const f32x2 r2 = {rstd, rstd}, n2 = {nb, nb};
    STAMP(ws, 0, 1);
    auto norm = [&](int i) {
      f32x2 f[4];
#pragma unroll
      for (int j = 0; j < 4; ++j) { const u32 v = xr[i][j]; f[j][0] = bf_lo(v); f[j][1] = bf_hi(v); }
#pragma unroll
      for (int j = 0; j < 4; ++j) f[j] = __builtin_elementwise_fma(f[j], r2, n2);
      u32x4 o;
#pragma unroll
      for (int j = 0; j < 4; ++j) o[j] = pk_bf16(f[j][0], f[j][1]);
      return o;
    };
    const int fw = (xrow >> 1) & 7;
    char* wA = lds + xrow * 384 + ((xj ^ fw) * 16);
    char* wB = lds + xrow * 384 + (((4 + xj) ^ fw) * 16);
    SB();
    auto nw = [&](auto I_) {
      constexpr int I = I_.value, th = I / 6, m = I % 6;
      *(u32x4*)(((m & 1) ? wB : wA) + th * 49152 + (m >> 1) * 128) = norm(I);
      if constexpr (I & 1) issue(IC<0>{}, IC<(I / 6)>{}, IC<4 + I / 6>{}, IC<(I >> 1) % 3>{});
      SB();
    };
    nw(IC<0>{}); nw(IC<1>{}); nw(IC<2>{}); nw(IC<3>{}); nw(IC<4>{}); nw(IC<5>{});
    nw(IC<6>{}); nw(IC<7>{}); nw(IC<8>{}); nw(IC<9>{}); nw(IC<10>{}); nw(IC<11>{});
    const int row = 32 * tgp + tok;
    const int f = (row >> 1) & 7;
    const char* rb = lds + row * 384;
#define RDX(TH, BASE)                                                                    \
    _Pragma("unroll") for (int s = 0; s < 12; ++s) {                                     \
      int c = 2 * s + h;                                                                 \
      int cp = (c & ~7) | ((c & 7) ^ f);                                                 \
      xa[TH * 12 + s] = *(const u32x4*)(rb + BASE + cp * 16);                            \
    }
    STAMP(ws, 0, 2);
    BAR();
    RDX(0, 0)
    RDX(1, 49152)
    SB();
#pragma unroll
    for (int i = 12; i < 18; ++i) xr[i] = norm(i);
    SB();
    BAR();
    STAMP(ws, 0, 3);
#pragma unroll
    for (int i = 12; i < 18; ++i) {
      const int m = i - 12;
      *(u32x4*)(((m & 1) ? wB : wA) + 49152 + (m >> 1) * 128) = xr[i];
    }
    BAR();
    RDX(2, 49152)
    SB();
    issue3(IC<0>{}, IC<2>{}, IC<0>{});
    issue3(IC<1>{}, IC<0>{}, IC<1>{});
    SB();
    WAIT_L0();
#undef RDX
  }
  STAMP(ws, 0, 4);

  const int q4 = (lane & 15) >> 2, p4 = lane & 3, g16 = (lane >> 4) & 1;
  const int pp = ZORI ? (((p4 & 1) << 1) | (p4 >> 1)) : p4;
  const int rd_off = (8 * h + q4) * 128 + (((4 * cs + 2 * g16 + (pp >> 1)) ^ ((q4 >> 1) << 2)) * 16) + (pp & 1) * 8;
  u32 rdb0 = LDS_ADDR(lds) + rd_off, rdb1 = rdb0 + 2 * K0_SLOT, rdb2 = rdb0 + 4 * K0_SLOT;
  asm volatile("" : "+v"(rdb0), "+v"(rdb1), "+v"(rdb2));
  char* dst0;
  size_t ustep;
  constexpr size_t PSTEP = ZORI ? (size_t)8 * 2048 * 16 : (size_t)64 * 16;
  if (ZORI) {
    dst0 = ws + OFF_Z1 + ((size_t)((cg * 384 + cs * 32) / 8 + h) * 2048 + t0 + 32 * tgp + tok) * 16;
    ustep = (size_t)2 * 2048 * 16;
  } else {
    const int col = (IS_Q ? FH + (cg - 12) * 384 : (cg - 6) * 384) + cs * 32 + (lane & 31);
    dst0 = ws + OFF_ZQ + ((size_t)(t0 / 8 + 4 * tgp + h) * NZQ + col) * 16;
    ustep = (size_t)2 * NZQ * 16;
  }
  u32x4 wf[4];
  auto rdf = [&](int slot, int s) {
    const u32 a = (slot < 2 ? rdb0 : slot < 4 ? rdb1 : rdb2) + (slot & 1) * K0_SLOT + s * 2048;
    const u32x2 lo = __builtin_bit_cast(u32x2, __builtin_amdgcn_ds_read_tr16_b64_v4i16((AS3 s16x4*)(uintptr_t)a));
    const u32x2 hi = __builtin_bit_cast(u32x2, __builtin_amdgcn_ds_read_tr16_b64_v4i16((AS3 s16x4*)(uintptr_t)(a + 512)));
    const u32x4 r = {lo[0], lo[1], hi[0], hi[1]};
    return r;
  };
  f32x2 ee;
  u32x4 eo;
  auto piece = [&](auto K_, const f32x16& pv, char* dst) {
    constexpr int K = K_.value, J = K >> 2, SUB = K & 3;
    const f32x2 v = {pv[2 * J], pv[2 * J + 1]};
    if constexpr (SUB == 0) {
      if (GELU) {
        const float c0 = -2.0f * 0.7978845608028654f * 1.4426950408889634f;
        const float c1 = c0 * 0.044715f;
        const f32x2 c0v = {c0, c0}, c1v = {c1, c1};
        ee = v * __builtin_elementwise_fma(v * v, c1v, c0v);
      }
    } else if constexpr (SUB == 1) {
      if (GELU) { ee[0] = fexp2(ee[0]); ee[1] = fexp2(ee[1]); }
    } else if constexpr (SUB == 2) {
      if (GELU) {
        const f32x2 one = {1.f, 1.f};
        const f32x2 d = ee + one;
        ee[0] = frcp(d[0]); ee[1] = frcp(d[1]);
      }
    } else {
      if (GELU) {
        const f32x2 o = v * ee;
        eo[J & 3] = pk_bf16(o[0], o[1]);
      } else {
        eo[J & 3] = pk_bf16(v[0], v[1]);
      }
      if ((J & 3) == 3) st128<AUX0>(srdO, ws, dst + (J >> 2) * ustep, eo);
    }
  };
  auto pair = [&](auto P_, auto SPL_, f32x16& acc, const f32x16& pv) {
    constexpr int p = P_.value, ODD = p & 1, PREV = p > 0, SPL = SPL_.value;
    constexpr int NS = SPL ? 6 : 12, S0 = SPL ? 6 * (SPL - 1) : 0;
    char* dst = dst0 + (size_t)(p - 1) * PSTEP;
#pragma unroll
    for (int i = 0; i < 16; ++i) acc[i] = 0.f;
    auto stage = [&](auto TH_) {
      constexpr int TH = TH_.value;
      constexpr int SL = (3 * ODD + TH + 4) % 6, SN = (SL + 1) % 6, SI = (SL + 5) % 6;
      constexpr int ITH = (TH + 2) % 3;
      constexpr bool FIRST = !PREV && TH == 0;
      constexpr int ip = p + (TH == 0 ? 1 : 2);
      if (FIRST || (SPL && TH == 1)) WAIT_V(6); else if (!(SPL && TH == 2)) WAIT_V(9);
      __builtin_amdgcn_s_barrier();
      SB();
      if ((!PREV || SPL) && TH == 0) { wf[0] = rdf(SL, S0); wf[1] = rdf(SL, S0 + 1); wf[2] = rdf(SL, S0 + 2); SB(); }
      auto step = [&](auto J_) {
        constexpr int J = J_.value, S = S0 + J, G = TH * NS + J;
        if constexpr (J < NS) {
          if (ZORI) acc = mfma32(wf[G & 3], xa[TH * 12 + S], acc);
          else      acc = mfma32(xa[TH * 12 + S], wf[G & 3], acc);
          SB();
          if (!(SPL && TH == 2 && J + 3 >= NS)) wf[(G + 3) & 3] = (J + 3 < NS) ? rdf(SL, S + 3) : rdf(SN, S0 + J + 3 - NS);
          if constexpr (!SPL) {
            if constexpr (FIRST && (S == 0 || S == 2 || S == 4)) issue(IC<1>{}, IC<1>{}, IC<2>{}, IC<S / 2>{});
            if constexpr (S == (FIRST ? 6 : 1)) issue(IC<ip>{}, IC<ITH>{}, IC<SI>{}, IC<0>{});
            if constexpr (S == (FIRST ? 8 : 5)) issue(IC<ip>{}, IC<ITH>{}, IC<SI>{}, IC<1>{});
            if constexpr (S == (FIRST ? 10 : 9)) issue(IC<ip>{}, IC<ITH>{}, IC<SI>{}, IC<2>{});
            if constexpr (S == 11 && PREV) pf(IC<(p - 1) * 3 + TH>{});
          }
          if constexpr (PREV && !SPL && G >= K0_G0 && G < 32 + K0_G0) piece(IC<G - K0_G0>{}, pv, dst);
          if constexpr (PREV && SPL && G < 16) { piece(IC<2 * G>{}, pv, dst); piece(IC<2 * G + 1>{}, pv, dst); }
          SB();
        }
      };
      step(IC<0>{}); step(IC<1>{}); step(IC<2>{}); step(IC<3>{}); step(IC<4>{}); step(IC<5>{});
      step(IC<6>{}); step(IC<7>{}); step(IC<8>{}); step(IC<9>{}); step(IC<10>{}); step(IC<11>{});
    };
    stage(IC<0>{}); stage(IC<1>{}); stage(IC<2>{});
  };
  f32x16 accA, accB;
  pair(IC<0>{}, IC<0>{}, accA, accA);
  pair(IC<1>{}, IC<0>{}, accB, accA);
  pair(IC<2>{}, IC<0>{}, accA, accB);
  pair(IC<3>{}, IC<0>{}, accB, accA);
  pair(IC<4>{}, IC<0>{}, accA, accB);
  pair(IC<5>{}, IC<0>{}, accB, accA);
  STAMP(ws, 0, 5);
  if (NP7) {
    if ((single & 1) != cs) { rdb0 ^= 64; rdb1 ^= 64; rdb2 ^= 64; }
    char* xch = lds + 4 * K0_SLOT + lane * 16;
    char* dst = ws + OFF_ZQ + ((size_t)(t0 / 8 + 4 * tgp + h) * NZQ + FH + single * 32 + (lane & 31)) * 16;
    auto tail = [&](auto CS_) {
      constexpr int CS = CS_.value;
      pair(IC<6>{}, IC<1 + CS>{}, accA, accB);
      f32x4 s0, s1;
#pragma unroll
      for (int j = 0; j < 4; ++j) { s0[j] = accA[8 * (1 - CS) + j]; s1[j] = accA[8 * (1 - CS) + 4 + j]; }
      *(f32x4*)(xch + w * 2048) = s0;
      *(f32x4*)(xch + w * 2048 + 1024) = s1;
      BAR();
      s0 = *(const f32x4*)(xch + (w ^ 4) * 2048);
      s1 = *(const f32x4*)(xch + (w ^ 4) * 2048 + 1024);
      u32x4 o;
      o[0] = pk_bf16(accA[8 * CS + 0] + s0[0], accA[8 * CS + 1] + s0[1]);
      o[1] = pk_bf16(accA[8 * CS + 2] + s0[2], accA[8 * CS + 3] + s0[3]);
      o[2] = pk_bf16(accA[8 * CS + 4] + s1[0], accA[8 * CS + 5] + s1[1]);
      o[3] = pk_bf16(accA[8 * CS + 6] + s1[2], accA[8 * CS + 7] + s1[3]);
      st128<AUX0>(srdO, ws, dst + CS * ustep, o);
    };
    if (cs == 0) tail(IC<0>{}); else tail(IC<1>{});
  } else {
    char* dst = dst0 + 5 * PSTEP;
#pragma unroll
    for (int u = 0; u < 2; ++u) {
      u32x4 o;
#pragma unroll
      for (int j = 0; j < 4; ++j)
        o[j] = GELU ? pk_bf16(gelu_tanh(accB[8 * u + 2 * j]), gelu_tanh(accB[8 * u + 2 * j + 1])) : pk_bf16(accB[8 * u + 2 * j], accB[8 * u + 2 * j + 1]);
      st128<AUX0>(srdO, ws, dst + u * ustep, o);
    }
  }
  WAIT_V(0);
  STAMP(ws, 0, 6);
}

__global__ __launch_bounds__(512, 2) void k0_inproj(const u16* __restrict__ x, const u16* __restrict__ wqkv,
                                                    const u16* __restrict__ w_in, const u16* __restrict__ w_sp,
                                                    const u16* __restrict__ w_out, const u16* __restrict__ wo, char* __restrict__ ws) {
  __shared__ __attribute__((aligned(16))) char lds[6 * K0_SLOT + 8192];
  const int b = blockIdx.x, xcd = b & 7, idx = b >> 3;
  const int cg = xcd * 2 + (idx & 1), rt = idx >> 1;
  CODE_WARM("_Z6k1_midPKtS0_Pc", 2, 9216, LDS_ADDR(lds) + 6 * K0_SLOT + 7 * 1024);
  if (cg < 6)       k0_body<1, 1, 0>(lds, x, wqkv, w_in, w_sp, w_out, wo, ws, rt, cg);
  else if (cg < 12) k0_body<0, 1, 1>(lds, x, wqkv, w_in, w_sp, w_out, wo, ws, rt, cg);
  else              k0_body<0, 0, 0>(lds, x, wqkv, w_in, w_sp, w_out, wo, ws, rt, cg);
}

constexpr int AT_TG = 320;
constexpr int AT_K = 0, AT_V = 64 * AT_TG, AT_Q = 2 * 64 * AT_TG, AT_ONES = AT_Q + 32 * AT_TG;

constexpr int AT_TR = ((AT_ONES + 16 + 512 + 127) / 128) * 128;
constexpr int AT_RED = AT_ONES + 16;
template <int CTRL> DEVI float dpp_mov(float v) {
  return __builtin_bit_cast(float, __builtin_amdgcn_update_dpp(0, __builtin_bit_cast(int, v), CTRL, 0xf, 0xf, true));
}
DEVI float sum32(float a, float b) {
  auto r = __builtin_amdgcn_permlane32_swap(__builtin_bit_cast(u32, a), __builtin_bit_cast(u32, b), false, false);
  u32 r0 = r[0], r1 = r[1];
  return __builtin_bit_cast(float, r0) + __builtin_bit_cast(float, r1);
}
DEVI float sum16(float a, float b) {
  auto r = __builtin_amdgcn_permlane16_swap(__builtin_bit_cast(u32, a), __builtin_bit_cast(u32, b), false, false);
  u32 r0 = r[0], r1 = r[1];
  return __builtin_bit_cast(float, r0) + __builtin_bit_cast(float, r1);
}

__global__ __launch_bounds__(512, 2) void k1_mid(const u16* __restrict__ w_out, const u16* __restrict__ wo, char* __restrict__ ws) {
  __shared__ __attribute__((aligned(16))) char lds[AT_TR + 16384];
  const int b = blockIdx.x;
  const int head = (b & 7) * 4 + ((b >> 3) & 3), qb = b >> 5;
  const int tid = threadIdx.x, lane = tid & 63;
  const int w = __builtin_amdgcn_readfirstlane(tid >> 6);
  const int h = lane >> 5;
  STAMP(ws, 1, 0);
  const srd_t srdO = MAKE_SRD(ws);
  CODE_WARM("_Z10k2_spatialPKtS0_Pc", 2, 11520, LDS_ADDR(lds) + AT_TR);
  const int tl = tid - 256, g = b * 102 + tl;
  const bool tile_on = (tl >= 0) && (tl < 102) && (g < 360 * 72);
  const int fc = g / 72, dq = g - fc * 72;
  u32x4 tr[8];
#pragma unroll
  for (int i = 0; i < 8; ++i) tr[i] = u32x4{0u, 0u, 0u, 0u};
  if (tile_on) {
    const char* src = (fc < 288) ? (const char*)w_out + (size_t)fc * 8 * 1152 : (const char*)wo + (size_t)(fc - 288) * 8 * 1152;
    src += dq * 16;
#pragma unroll
    for (int i = 0; i < 8; ++i) tr[i] = *(const u32x4*)(src + i * 1152);
  }
  auto tile_store = [&]() {
    if (w == 4 || w == 5) {
      char* tl_ = lds + AT_TR + (w - 4) * 8192;
#pragma unroll
      for (int d = 0; d < 8; ++d) {
        u32x4 o;
#pragma unroll
        for (int m = 0; m < 4; ++m)
          o[m] = __builtin_amdgcn_perm(tr[2 * m + 1][d >> 1], tr[2 * m][d >> 1], (d & 1) ? 0x07060302u : 0x05040100u);
        *(u32x4*)(tl_ + lane * 128 + ((d ^ (lane & 7)) * 16)) = o;
      }
      const int g0 = b * 102 + (w - 4) * 64;
      int nv = 102 - (w - 4) * 64;
      nv = nv > 64 ? 64 : nv;
      nv = nv < 360 * 72 - g0 ? nv : 360 * 72 - g0;
      nv = __builtin_amdgcn_readfirstlane(nv < 0 ? 0 : nv);
      char* dst0 = ws + OFF_WT + (size_t)__builtin_amdgcn_readfirstlane(g0) * 128;
      const srd_t srdT = MAKE_SRDN(dst0, nv * 128);
#pragma unroll
      for (int d = 0; d < 8; ++d) {
        const int row = d * 8 + (lane >> 3);
        const u32x4 o = *(const u32x4*)(tl_ + row * 128 + (((lane & 7) ^ (row & 7)) * 16));
        st128<AUXT>(srdT, dst0, dst0 + d * 1024 + lane * 16, o);
      }
    }
  };
  u32x4 zv[5];
  {
    const srd_t srz = MAKE_SRDN(ws + OFF_ZQ + (size_t)b * NZQ * 16, FH * 16);
    const u32 vo = (u32)tid * 16u;
    asm volatile("buffer_load_dwordx4 %0, %5, %6, 0 offen\n\t"
                 "buffer_load_dwordx4 %1, %5, %6, %7 offen\n\t"
                 "buffer_load_dwordx4 %2, %5, %6, %8 offen\n\t"
                 "buffer_load_dwordx4 %3, %5, %6, %9 offen\n\t"
                 "buffer_load_dwordx4 %4, %10, %6, 0 offen"
                 : "=&v"(zv[0]), "=&v"(zv[1]), "=&v"(zv[2]), "=&v"(zv[3]), "=&v"(zv[4])
                 : "v"(vo), "s"(srz), "s"(8192u), "s"(16384u), "s"(24576u), "v"(vo + 32768u)
                 : "memory");
  }
  {
    const srd_t srd = MAKE_SRD(ws + OFF_ZQ);
    const u32 l0 = LDS_ADDR(lds);
    const int tgk0 = 32 * qb - 32;
#pragma unroll
    for (int it = 0; it < 7; ++it) {
      const int n = w + 8 * it;
      if (it < 6 || w < 2) {
        const int part = n < 20 ? 1 : n < 40 ? 2 : 0;
        const int nn = n < 20 ? n : n < 40 ? n - 20 : n - 40;
        const int i = nn * 64 + lane;
        const int tg = i / 20;
        int d = i - tg * 20;
        d = d > 17 ? 17 : d;
        int gtg = (part == 0 ? 32 * qb : tgk0) + tg;
        gtg = gtg < 0 ? 0 : gtg;
        const u32 voff = (u32)(gtg * NZQ + d) * 16u;
        const u32 soff = (u32)(FH + 576 * part + 18 * head) * 16u;
        bufl(l0 + (part == 1 ? AT_K : part == 2 ? AT_V : AT_Q) + nn * 1024, voff, srd, soff);
      }
    }
  }
  if (tid == 0) { u32x4 o = {0x3f803f80u, 0x3f803f80u, 0x3f803f80u, 0x3f803f80u}; *(u32x4*)(lds + AT_ONES) = o; }
  asm volatile("s_waitcnt vmcnt(6)" : "+v"(zv[0]), "+v"(zv[1]), "+v"(zv[2]), "+v"(zv[3]), "+v"(zv[4])::"memory");
  f32x2 zx[5][4];
  {
    f32x2 S[4], Q[4];
#pragma unroll
    for (int e = 0; e < 4; ++e) {
#pragma unroll
      for (int i = 0; i < 5; ++i) {
        const u32 u = zv[i][e];
        zx[i][e] = f32x2{bf_lo(u), bf_hi(u)};
        if (i == 0) { S[e] = zx[0][e]; Q[e] = zx[0][e] * zx[0][e]; }
        else { S[e] += zx[i][e]; Q[e] = __builtin_elementwise_fma(zx[i][e], zx[i][e], Q[e]); }
      }
    }
    float r8[8], u4[4];
#pragma unroll
    for (int e = 0; e < 8; ++e) { const float a = S[e >> 1][e & 1], q = Q[e >> 1][e & 1]; r8[e] = sum32(a, q); }
#pragma unroll
    for (int e = 0; e < 4; ++e) {
      float u = sum16(r8[e], r8[e + 4]);
      u += dpp_mov<0x128>(u); u += dpp_mov<0x124>(u); u += dpp_mov<0x122>(u); u += dpp_mov<0x121>(u);
      u4[e] = u;
    }
    if ((lane & 15) == 0) *(f32x4*)(lds + AT_RED + w * 64 + (lane >> 4) * 16) = f32x4{u4[0], u4[1], u4[2], u4[3]};
  }
  __builtin_amdgcn_s_waitcnt(0x0F70);
  WAIT_V(0);
  BAR();
  STAMP(ws, 1, 1);
  if (TILE_POS == 0) tile_store();
  {
    const float* red = (const float*)(lds + AT_RED) + (lane & 15);
    float tot = 0.f;
#pragma unroll
    for (int i = 0; i < 8; ++i) tot += red[16 * i];
    const float q = dpp_mov<0x128>(tot);
    const float mean = tot * (1.0f / 2304.0f);
    const float var = fmaxf(__builtin_fmaf(-mean, mean, q * (1.0f / 2304.0f)), 0.f);
    const float rs = __builtin_amdgcn_rsqf(var + 1e-5f), nb = -mean * rs;
    f32x2 rs2[4], nb2[4];
    rs2[0] = f32x2{dpp_mov<0x150>(rs), dpp_mov<0x151>(rs)}; nb2[0] = f32x2{dpp_mov<0x150>(nb), dpp_mov<0x151>(nb)};
    rs2[1] = f32x2{dpp_mov<0x152>(rs), dpp_mov<0x153>(rs)}; nb2[1] = f32x2{dpp_mov<0x152>(nb), dpp_mov<0x153>(nb)};
    rs2[2] = f32x2{dpp_mov<0x154>(rs), dpp_mov<0x155>(rs)}; nb2[2] = f32x2{dpp_mov<0x154>(nb), dpp_mov<0x155>(nb)};
    rs2[3] = f32x2{dpp_mov<0x156>(rs), dpp_mov<0x157>(rs)}; nb2[3] = f32x2{dpp_mov<0x156>(nb), dpp_mov<0x157>(nb)};
    char* dst = ws + OFF_ZN + ((size_t)b * FH + tid) * 16;
#pragma unroll
    for (int i = 0; i < 5; ++i) {
      u32x4 o;
#pragma unroll
      for (int e = 0; e < 4; ++e) {
        const f32x2 y = __builtin_elementwise_fma(zx[i][e], rs2[e], nb2[e]);
        o[e] = pk_bf16(y[0], y[1]);
      }
      if (i < 4 || w < 4) st128<AUX1>(srdO, ws, dst + i * 8192, o);
    }
  }
  if (TILE_POS == 1) tile_store();
  SB();
  STAMP(ws, 1, 2);
  const int q4 = (lane & 15) >> 2, p4 = lane & 3, g16 = (lane >> 4) & 1;
  u32x4 qf0, qf1;
  {
    const char* qb_ = lds + AT_Q + (4 * w + 2 * g16 + (p4 >> 1)) * AT_TG + (p4 & 1) * 8;
    int d0 = 8 * h + q4;
    qf0 = tr_read2(qb_ + d0 * 16, qb_ + (d0 + 4) * 16);
    int d1 = 16 + q4; d1 = d1 > 17 ? 17 : d1;
    u32x2 t = tr_read(qb_ + d1 * 16);
    qf1[0] = h ? 0u : t[0]; qf1[1] = 0u; qf1[2] = 0u; qf1[3] = 0u;
  }
  const char* kb0 = lds + AT_K + (4 * w + 2 * g16 + (p4 & 1)) * AT_TG + (p4 >> 1) * 8;
  const int kd0 = (8 * h + q4) * 16;
  const int kd1 = (16 + q4 > 17 ? 17 : 16 + q4) * 16;
  const int n = lane & 31;
  const float NEG = -3.0e38f;
  f32x16 s[9];
#pragma unroll
  for (int jj = 0; jj < 9; ++jj) {
    const char* kb = kb0 + jj * 4 * AT_TG;
    u32x4 k0 = tr_read2(kb + kd0, kb + kd0 + 64);
    u32x2 k1a = tr_read(kb + kd1);
    u32x4 k1 = {k1a[0], k1a[1], k1a[0], k1a[1]};
    f32x16 z;
#pragma unroll
    for (int i = 0; i < 16; ++i) z[i] = 0.f;
    s[jj] = mfma32(k1, qf1, mfma32(k0, qf0, z));
  }
  if (qb == 0) {
#pragma unroll
    for (int jj = 0; jj < 8; ++jj)
      if (jj < 8 - w) {
        asm volatile("" ::: "memory");
#pragma unroll
        for (int r = 0; r < 16; ++r) s[jj][r] = NEG;
      }
  }
  u32 mk[8];
  {
    const int nb = n - 8 * h;
#pragma unroll
    for (int j = 0; j < 8; ++j) {
      const int k0 = ((2 * j) & 7) + 16 * (j >> 2);
      const bool c0 = k0 <= nb, c1 = k0 + 1 <= nb;
      s[0][2 * j] = c0 ? s[8][2 * j] : s[0][2 * j];
      s[0][2 * j + 1] = c1 ? s[8][2 * j + 1] : s[0][2 * j + 1];
      mk[j] = c1 ? 0xffffffffu : (c0 ? 0x0000ffffu : 0u);
    }
  }
  float m = __builtin_fmaxf(s[0][0], s[0][1]);
#pragma unroll
  for (int jj = 0; jj < 8; ++jj)
#pragma unroll
    for (int r = (jj == 0 ? 2 : 0); r < 16; r += 2) m = __builtin_fmaxf(__builtin_fmaxf(m, s[jj][r]), s[jj][r + 1]);
  {
    auto r = __builtin_amdgcn_permlane32_swap(__builtin_bit_cast(u32, m), __builtin_bit_cast(u32, m), false, false);
    u32 r0 = r[0], r1 = r[1];
    m = __builtin_fmaxf(__builtin_bit_cast(float, r0), __builtin_bit_cast(float, r1));
  }
  STAMP(ws, 1, 3);
  const float c = 0.23570226039551584f * 1.4426950408889634f;
  const f32x2 c2 = {c, c}, mc2 = {-m * c, -m * c};
  f32x16 o;
#pragma unroll
  for (int i = 0; i < 16; ++i) o[i] = 0.f;
  const char* vb0 = (n == 18) ? lds + AT_ONES : lds + AT_V + (4 * w + h) * AT_TG + n * 16;
  const int vstep = (n == 18) ? 0 : AT_TG;
  u32x4 va = *(const u32x4*)(vb0 + 4 * vstep), vb = *(const u32x4*)(vb0 + 6 * vstep);
#pragma unroll
  for (int jj = 1; jj <= 8; ++jj) {
    const int tt = jj & 7;
    const int nx = jj < 8 ? jj + 1 : 0;
    const u32x4 van = *(const u32x4*)(vb0 + (4 * nx) * vstep), vbn = *(const u32x4*)(vb0 + (4 * nx + 2) * vstep);
    u32x4 p0, p1;
#pragma unroll
    for (int j = 0; j < 4; ++j) {
      const f32x2 a = __builtin_elementwise_fma(f32x2{s[tt][2 * j], s[tt][2 * j + 1]}, c2, mc2);
      const f32x2 bq = __builtin_elementwise_fma(f32x2{s[tt][8 + 2 * j], s[tt][8 + 2 * j + 1]}, c2, mc2);
      p0[j] = pk_bf16(fexp2(a[0]), fexp2(a[1]));
      p1[j] = pk_bf16(fexp2(bq[0]), fexp2(bq[1]));
    }
    if (jj < 8) {
      o = mfma32(va, p0, o);
      o = mfma32(vb, p1, o);
    } else {
      u32x4 m0, m1;
#pragma unroll
      for (int j = 0; j < 4; ++j) { m0[j] = p0[j] & mk[j]; m1[j] = p1[j] & mk[4 + j]; }
      o = mfma32(va, m0, o);
      o = mfma32(vb, m1, o);
#pragma unroll
      for (int j = 0; j < 4; ++j) { m0[j] ^= p0[j]; m1[j] ^= p1[j]; }
      o = mfma32(van, m0, o);
      o = mfma32(vbn, m1, o);
    }
    va = van; vb = vbn;
    SB();
  }
  STAMP(ws, 1, 4);
  float l = o[10];
  {
    auto r = __builtin_amdgcn_permlane32_swap(__builtin_bit_cast(u32, l), __builtin_bit_cast(u32, l), false, false);
    u32 r0 = r[0];
    l = __builtin_bit_cast(float, r0);
  }
  const float inv = 1.0f / l;
  const int t = 256 * qb + 32 * w + n;
  char* po = ws + OFF_PO + (size_t)t * 16;
  const int cb = 18 * head + 4 * h;
#pragma unroll
  for (int pr = 0; pr < 5; ++pr) {
    const int r = 2 * pr;
    const int d = (r & 3) + 8 * (r >> 2);
    if (pr < 4 || h == 0) {
      int cc = cb + d;
      u32 v = pk_bf16(o[r] * inv, o[r + 1] * inv);
      st32<AUXO>(srdO, ws, po + (size_t)(288 + (cc >> 3)) * 2048 * 16 + (cc & 7) * 2, v);
    }
  }
  if (TILE_POS == 2) tile_store();
  STAMP(ws, 1, 5);
}

constexpr int K2_SLOT = 16384 + 8 * 2304;

template <int EXTRA>
DEVI void k2_body(char* lds, const u16* __restrict__ wsp, const u16* __restrict__ bsp, char* __restrict__ ws) {
  const int tid = threadIdx.x, lane = tid & 63;
  const int w = __builtin_amdgcn_readfirstlane(tid >> 6);
  const int kh = w >> 2, tq = w & 3;
  const int b = blockIdx.x, xcd = b & 7, idx = b >> 3;
  const int tb = (xcd & 3) * 4 + (idx & 3), fb = (xcd >> 2) * 8 + (idx >> 2);
  const int t0 = tb * 128, f0 = fb * 144;
  STAMP(ws, 2, 0);

  u32 wv0, wv1, zv[3];
  {
    int r0 = 16 * w + (lane >> 3), r1 = r0 + 8;
    wv0 = (u32)(r0 * 4096 + (((lane & 7) ^ ((r0 >> 1) & 7)) * 16));
    wv1 = (u32)(r1 * 4096 + (((lane & 7) ^ ((r1 >> 1) & 7)) * 16));
#pragma unroll
    for (int j = 0; j < 3; ++j) {
      int e = (w + 8 * j) * 64 + lane;
      int tg = e / 144, f = e - tg * 144;
      zv[j] = (u32)((tg * FH + f) * 16);
    }
  }
  const srd_t wsrd = MAKE_SRD((const char*)wsp + (size_t)t0 * 4096);
  const srd_t zsrd = MAKE_SRD(ws + OFF_ZN + (size_t)f0 * 16);
  const u32 ldsb = LDS_ADDR(lds) + w * 1024;
  auto piece = [&](int it, int j) {
    const u32 sl = ldsb + (it & 3) * K2_SLOT;
    const u32 wo = it * 128, zo = it * (8 * FH * 16);
    if (j == 0) BUFL(sl + w * 1024, wv0, wsrd, wo);
    if (j == 1) BUFL(sl + 16384, zv[0], zsrd, zo);
    if (j == 2) BUFL(sl + w * 1024 + 1024, wv1, wsrd, wo);
    if (j == 3) BUFL(sl + 16384 + 8192, zv[1], zsrd, zo);
    if (j == 4 && EXTRA) BUFL(sl + 16384 + 16384, zv[2], zsrd, zo);
  };
  auto issue = [&](int it) { piece(it, 0); piece(it, 1); piece(it, 2); piece(it, 3); piece(it, 4); };
#define K2_WAIT(n) do { if (EXTRA) WAIT_V_(n * 5); else WAIT_V_(n * 4); } while (0)
#define K2_WAITZ(n) do { if (EXTRA) WAIT_V_(n * 5 + 12); else WAIT_V_(n * 4 + 12); } while (0)
  issue(0); issue(1); issue(2); issue(3);

  const int g = lane >> 4, l15 = lane & 15;
  f32x4 acc[9][2];
#pragma unroll
  for (int i = 0; i < 9; ++i) { acc[i][0] = f32x4{0.f, 0.f, 0.f, 0.f}; acc[i][1] = f32x4{0.f, 0.f, 0.f, 0.f}; }
  int woff[2];
#pragma unroll
  for (int ti = 0; ti < 2; ++ti) {
    int row = 32 * tq + 16 * ti + l15;
    woff[ti] = row * 128 + (((4 * kh + g) ^ ((row >> 1) & 7)) * 16);
  }
  const int zoff = 16384 + (4 * kh + g) * 2304 + l15 * 16;

  struct Frags { u32x4 a[9]; u32x4 b[2]; };
  auto rdb = [&](int it, Frags& F) {
    const char* sl = lds + (it & 3) * K2_SLOT;
    F.b[0] = *(const u32x4*)(sl + woff[0]);
    F.b[1] = *(const u32x4*)(sl + woff[1]);
  };
  auto rda = [&](int it, Frags& F, int fi) {
    const char* sl = lds + (it & 3) * K2_SLOT;
    F.a[fi] = *(const u32x4*)(sl + zoff + fi * 256);
  };
  auto mm1 = [&](const Frags& F, int fi) {
    acc[fi][0] = mfma16(F.a[fi], F.b[0], acc[fi][0]);
    acc[fi][1] = mfma16(F.a[fi], F.b[1], acc[fi][1]);
  };
  auto stage = [&](const Frags& C, Frags& N, int rit, int lit, auto LDc, auto RDc) {
    constexpr bool LD = decltype(LDc)::value, RD = decltype(RDc)::value;
    SB();
    if (RD) rdb(rit, N);
    mm1(C, 0); SB();
    if (RD) { rda(rit, N, 0); rda(rit, N, 1); }
    mm1(C, 1); if (LD) piece(lit, 0); SB();
    if (RD) { rda(rit, N, 2); rda(rit, N, 3); }
    mm1(C, 2); SB();
    if (RD) { rda(rit, N, 4); rda(rit, N, 5); }
    mm1(C, 3); if (LD) piece(lit, 1); SB();
    if (RD) { rda(rit, N, 6); rda(rit, N, 7); }
    mm1(C, 4); SB();
    if (RD) rda(rit, N, 8);
    mm1(C, 5); if (LD) piece(lit, 2); SB();
    mm1(C, 6); SB();
    mm1(C, 7); if (LD) piece(lit, 3); SB();
    mm1(C, 8); if (LD) piece(lit, 4); SB();
  };
  using LDT = std::true_type;
  using T = std::true_type; using Fa = std::false_type;
  Frags F0, F1;
  K2_WAIT(3); BAR();
  STAMP(ws, 2, 1);
  rdb(0, F0);
#pragma unroll
  for (int fi = 0; fi < 9; ++fi) rda(0, F0, fi);
  for (int it = 0; it < 28; it += 2) {
    K2_WAIT(2); BAR();
    stage(F0, F1, it + 1, it + 4, LDT{}, T{});
    K2_WAIT(2); BAR();
    stage(F1, F0, it + 2, it + 5, LDT{}, T{});
  }
  STAMP(ws, 2, 2);
  const size_t eoff = ((size_t)(f0 / 8 + (g >> 1)) * 2048 + t0 + 32 * tq + l15) * 16 + (g & 1) * 8;
  u32x2 z1v[5][2];
  u32 biasr[2];
  K2_WAIT(2); BAR();
#pragma unroll
  for (int fi = 0; fi < 5; ++fi) {
    const int fa = (kh == 0) ? fi : (fi < 4 ? 5 + fi : 8);
#pragma unroll
    for (int ti = 0; ti < 2; ++ti)
      z1v[fi][ti] = *(const u32x2*)(ws + OFF_Z1 + eoff + ((size_t)fa * 2 * 2048 + ti * 16) * 16);
  }
#pragma unroll
  for (int ti = 0; ti < 2; ++ti) biasr[ti] = (u32)bsp[t0 + 32 * tq + 16 * ti + l15];
  stage(F0, F1, 29, 0, Fa{}, T{});
  K2_WAITZ(1); BAR(); stage(F1, F0, 30, 0, Fa{}, T{});
  K2_WAITZ(0); BAR(); stage(F0, F1, 31, 0, Fa{}, T{});
  stage(F1, F0, 0, 0, Fa{}, Fa{});
  STAMP(ws, 2, 3);
  float bias[2] = {bf_lo(biasr[0]), bf_lo(biasr[1])};
  BAR();
  char* xch = lds + tq * 18432 + lane * 16;
  auto finish = [&](auto KH) {
    constexpr int khc = decltype(KH)::value;
    constexpr int lo = khc ? 5 : 0, hi = khc ? 9 : 5;
#pragma unroll
    for (int fi = 0; fi < 9; ++fi)
      if (fi < lo || fi >= hi) {
        *(f32x4*)(xch + (fi * 2 + 0) * 1024) = acc[fi][0];
        *(f32x4*)(xch + (fi * 2 + 1) * 1024) = acc[fi][1];
      }
    BAR();
#pragma unroll
    for (int fi = lo; fi < hi; ++fi)
#pragma unroll
      for (int ti = 0; ti < 2; ++ti) {
        f32x4 o = *(const f32x4*)(xch + (fi * 2 + ti) * 1024);
        f32x4 gq = acc[fi][ti] + o;
        u32x2 z = z1v[fi - lo][ti];
        u32x2 r;
        r[0] = pk_bf16(bf_lo(z[0]) * (gq[0] + bias[ti]), bf_hi(z[0]) * (gq[1] + bias[ti]));
        r[1] = pk_bf16(bf_lo(z[1]) * (gq[2] + bias[ti]), bf_hi(z[1]) * (gq[3] + bias[ti]));
        st64<AUX2>(MAKE_SRD(ws), ws, ws + OFF_PO + eoff + ((size_t)fi * 2 * 2048 + ti * 16) * 16, r);
      }
  };
  if (kh == 0) finish(std::integral_constant<int, 0>{});
  else         finish(std::integral_constant<int, 1>{});
  STAMP(ws, 2, 4);
}

__global__ __launch_bounds__(512, 2) void k2_spatial(const u16* __restrict__ wsp, const u16* __restrict__ bsp, char* __restrict__ ws) {
  __shared__ __attribute__((aligned(16))) char lds[4 * K2_SLOT + 1024];
  CODE_WARM("_Z6k3_outPKtPcPt", 2, 13824, LDS_ADDR(lds) + 4 * K2_SLOT);
  if (__builtin_amdgcn_readfirstlane(threadIdx.x >> 6) < 2) k2_body<1>(lds, wsp, bsp, ws);
  else                   k2_body<0>(lds, wsp, bsp, ws);
}

constexpr int K3_SLOT = 8192 + 9216;

template <int EXTRA>
DEVI void k3_body(char* lds, const u16* __restrict__ x, char* __restrict__ ws, u16* __restrict__ out) {
  const int tid = threadIdx.x, lane = tid & 63;
  const int w = __builtin_amdgcn_readfirstlane(tid >> 6);
  const int kh = w >> 2, th = (w >> 1) & 1, dh = (w & 1) ^ kh;
  const int b = blockIdx.x, xcd = b & 7, idx = b >> 3;
  const int tb = xcd * 4 + (idx & 3), db = idx >> 2;
  const int t0 = tb * 64, d0 = db * 72;
  STAMP(ws, 3, 0);

  u32 pv = (u32)((w * 2048 + lane) * 16), wv[2];
#pragma unroll
  for (int j = 0; j < 2; ++j) {
    int e = (w + 8 * j) * 64 + lane;
    int fc = e / 72, dp = e - fc * 72;
    int d = ((fc & 1) && dp < 64) ? (dp ^ 8) : dp;
    wv[j] = (u32)((fc * 576 + d) * 16);
  }
  const srd_t psrd = MAKE_SRD(ws + OFF_PO + (size_t)t0 * 16);
  const srd_t wsrd = MAKE_SRD(ws + OFF_WT + (size_t)d0 * 16);
  const u32 ldsb = LDS_ADDR(lds) + w * 1024;
  auto piece = [&](int it, int j) {
    const u32 sl = ldsb + (it & 7) * K3_SLOT;
    if (j == 0) BUFL(sl, pv, psrd, it * (8 * 2048 * 16));
    if (j == 1) BUFL(sl + 8192, wv[0], wsrd, it * (8 * 576 * 16));
    if (j == 2 && EXTRA) BUFL(sl + 8192 + 8192, wv[1], wsrd, it * (8 * 576 * 16));
  };
  auto issue = [&](int it) { piece(it, 0); piece(it, 1); piece(it, 2); };
#define K3_WAIT(n, z) do { if (EXTRA) WAIT_V_((n) * 3 + (z)); else WAIT_V_((n) * 2 + (z)); } while (0)
#pragma unroll
  for (int i = 0; i < 8; ++i) issue(i);

  const int g = lane >> 4, l15 = lane & 15;
  const int poff = (4 * kh + g) * 1024 + (32 * th + l15) * 16;
  int aoff[3];
#pragma unroll
  for (int di = 0; di < 3; ++di) {
    int d = 48 * dh + 16 * di + l15;
    if (d >= 72) d = 64 + (l15 & 7);
    int dp = ((g & 1) && d < 64) ? (d ^ 8) : d;
    aoff[di] = 8192 + ((4 * kh + g) * 72 + dp) * 16;
  }
  f32x4 accB[3][2], accS[3][2];
#pragma unroll
  for (int i = 0; i < 3; ++i)
#pragma unroll
    for (int j = 0; j < 2; ++j) { accB[i][j] = f32x4{0.f, 0.f, 0.f, 0.f}; accS[i][j] = f32x4{0.f, 0.f, 0.f, 0.f}; }

  struct Frags { u32x4 a[3]; u32x4 b[2]; };
  auto body = [&](auto DHc) {
    constexpr int DH = decltype(DHc)::value;
    constexpr int NA = DH ? 2 : 3;
    auto rdb = [&](int it, Frags& F) {
      const char* sl = lds + (it & 7) * K3_SLOT;
      F.b[0] = *(const u32x4*)(sl + poff);
      F.b[1] = *(const u32x4*)(sl + poff + 256);
    };
    auto rda = [&](int it, Frags& F) {
      const char* sl = lds + (it & 7) * K3_SLOT;
#pragma unroll
      for (int di = 0; di < NA; ++di) F.a[di] = *(const u32x4*)(sl + aoff[di]);
    };
    auto mm1 = [&](const Frags& F, f32x4 (&acc)[3][2], int di) {
      acc[di][0] = mfma16(F.a[di], F.b[0], acc[di][0]);
      acc[di][1] = mfma16(F.a[di], F.b[1], acc[di][1]);
    };
    auto stage = [&](const Frags& C, Frags& N, f32x4 (&acc)[3][2], int rit, int lit, auto LDc, auto RDc) {
      constexpr bool LD = decltype(LDc)::value, RD = decltype(RDc)::value;
      SB();
      if (RD) rdb(rit, N);
      mm1(C, acc, 0); if (LD) piece(lit, 0); SB();
      if (RD) rda(rit, N);
      mm1(C, acc, 1); if (LD) piece(lit, 1); SB();
      if (NA == 3) mm1(C, acc, 2);
      if (LD) piece(lit, 2);
      SB();
    };
    using LDT = std::true_type;
    using T = std::true_type; using Fa = std::false_type;
    Frags F0, F1;
    K3_WAIT(7, 0); BAR();
    STAMP(ws, 3, 1);
    rdb(0, F0); rda(0, F0);
    for (int it = 0; it < 36; it += 2) {
      K3_WAIT(6, 0); BAR();
      stage(F0, F1, accB, it + 1, it + 8, LDT{}, T{});
      K3_WAIT(6, 0); BAR();
      stage(F1, F0, accB, it + 2, it + 9, LDT{}, T{});
    }
    STAMP(ws, 3, 2);
    K3_WAIT(6, 0); BAR();
    stage(F0, F1, accS, 37, 44, LDT{}, T{});
    const int t = t0 + 32 * th + 16 * kh + l15;
    u32x2 xv[3];
#pragma unroll
    for (int di = 0; di < 3; ++di) {
      int d = 48 * dh + 16 * di + 4 * g;
      d = d < 72 ? d : 0;
      xv[di] = *(const u32x2*)(x + (size_t)t * 576 + d0 + d);
    }
    K3_WAIT(6, 3); BAR(); stage(F1, F0, accS, 38, 0, Fa{}, T{});
    K3_WAIT(5, 3); BAR(); stage(F0, F1, accS, 39, 0, Fa{}, T{});
    K3_WAIT(4, 3); BAR(); stage(F1, F0, accS, 40, 0, Fa{}, T{});
    K3_WAIT(3, 3); BAR(); stage(F0, F1, accS, 41, 0, Fa{}, T{});
    K3_WAIT(2, 3); BAR(); stage(F1, F0, accS, 42, 0, Fa{}, T{});
    K3_WAIT(1, 3); BAR(); stage(F0, F1, accS, 43, 0, Fa{}, T{});
    K3_WAIT(0, 3); BAR(); stage(F1, F0, accS, 44, 0, Fa{}, T{});
    stage(F0, F1, accS, 0, 0, Fa{}, Fa{});
    asm volatile("" ::"v"(xv[0]), "v"(xv[1]), "v"(xv[2]));
    STAMP(ws, 3, 3);
    BAR();
    char* xch = lds + ((th * 2 + dh) * 2) * 6144 + lane * 16;
    auto finish = [&](auto KH) {
      constexpr int own = decltype(KH)::value, oth = own ^ 1;
#pragma unroll
      for (int di = 0; di < NA; ++di) {
        *(f32x4*)(xch + oth * 6144 + di * 1024) = accB[di][oth];
        *(f32x4*)(xch + oth * 6144 + 3072 + di * 1024) = accS[di][oth];
      }
      BAR();
#pragma unroll
      for (int di = 0; di < NA; ++di) {
        f32x4 Bv = accB[di][own] + *(const f32x4*)(xch + own * 6144 + di * 1024);
        f32x4 Sv = accS[di][own] + *(const f32x4*)(xch + own * 6144 + 3072 + di * 1024);
        int d = 48 * dh + 16 * di + 4 * g;
        if (d < 72) {
          u32x2 r;
          r[0] = pk_bf16(bf_lo(xv[di][0]) + Sv[0] * silu_f(Bv[0]), bf_hi(xv[di][0]) + Sv[1] * silu_f(Bv[1]));
          r[1] = pk_bf16(bf_lo(xv[di][1]) + Sv[2] * silu_f(Bv[2]), bf_hi(xv[di][1]) + Sv[3] * silu_f(Bv[3]));
          st64<AUX3>(MAKE_SRD(out), (const char*)out, (const char*)(out + (size_t)t * 576 + d0 + d), r);
        }
      }
    };
    if (kh == 0) finish(std::integral_constant<int, 0>{});
    else         finish(std::integral_constant<int, 1>{});
    STAMP(ws, 3, 4);
  };
  if (dh == 0) body(std::integral_constant<int, 0>{});
  else         body(std::integral_constant<int, 1>{});
}

__global__ __launch_bounds__(512, 2) void k3_out(const u16* __restrict__ x, char* __restrict__ ws, u16* __restrict__ out) {
  __shared__ __attribute__((aligned(16))) char lds[8 * K3_SLOT];
  if (__builtin_amdgcn_readfirstlane(threadIdx.x >> 6) == 0) k3_body<1>(lds, x, ws, out);
  else                                                       k3_body<0>(lds, x, ws, out);
}

static inline void launch_stage(int st, void* const* d_in, void* d_out, void* d_ws, hipStream_t stream) {
  const u16* x = (const u16*)d_in[0];
  const u16* wqkv = (const u16*)d_in[1];
  const u16* wo = (const u16*)d_in[2];
  const u16* w_in = (const u16*)d_in[4];
  const u16* w_sp = (const u16*)d_in[5];
  const u16* b_sp = (const u16*)d_in[6];
  const u16* w_out = (const u16*)d_in[7];
  char* ws = (char*)d_ws;
  if (st == 0) k0_inproj<<<256, 512, 0, stream>>>(x, wqkv, w_in, w_sp, w_out, wo, ws);
  if (st == 1) k1_mid<<<256, 512, 0, stream>>>(w_out, wo, ws);
  if (st == 2) k2_spatial<<<256, 512, 0, stream>>>(w_sp, b_sp, ws);
  if (st == 3) k3_out<<<256, 512, 0, stream>>>(x, ws, (u16*)d_out);
}

extern "C" void kernel_launch(void* const* d_in, const int* in_sizes, int n_in, void* d_out, int out_size, void* d_ws,
                              size_t ws_size, hipStream_t stream) {
  (void)in_sizes; (void)n_in; (void)out_size; (void)ws_size;
  for (int st = 0; st < 4; ++st) launch_stage(st, d_in, d_out, d_ws, stream);
}
```

```cpp
#include <hip/hip_runtime.h>
#include <hip/hip_bf16.h>
#include <stdint.h>
#include <type_traits>

typedef unsigned int u32;
typedef unsigned short u16;
typedef __attribute__((ext_vector_type(8))) __bf16 bf16x8;
typedef __attribute__((ext_vector_type(2))) __bf16 bf16x2;
typedef __attribute__((ext_vector_type(4))) short s16x4;
typedef __attribute__((ext_vector_type(2))) float f32x2;
typedef __attribute__((ext_vector_type(4))) float f32x4;
typedef __attribute__((ext_vector_type(16))) float f32x16;
typedef __attribute__((ext_vector_type(2))) u32 u32x2;
typedef __attribute__((ext_vector_type(4))) u32 u32x4;

#define DEVI __device__ __forceinline__
#define AS1 __attribute__((address_space(1)))
#define AS3 __attribute__((address_space(3)))
#define GLDS(g, l) __builtin_amdgcn_global_load_lds((const AS1 void*)(g), (AS3 void*)(l), 16, 0, 0)
#define WAIT_V(n) asm volatile("s_waitcnt vmcnt(" #n ")" ::: "memory")
#define WAIT_V_(n) asm volatile("s_waitcnt vmcnt(%0)" ::"n"(n) : "memory")
#define SB() __builtin_amdgcn_sched_barrier(0)
typedef __amdgpu_buffer_rsrc_t srd_t;
#define BUFL(m0v, voff, srd, soff) \
  asm volatile("s_mov_b32 m0, %0\n\ts_nop 0\n\tbuffer_load_dwordx4 %1, %2, %3 offen lds" ::"s"(m0v), "v"(voff), "s"(srd), "s"(soff) : "m0", "memory")
#define MAKE_SRD(p) __builtin_amdgcn_make_buffer_rsrc((void*)(p), (short)0, 0x7fffffff, 0x00020000)
#define LDS_ADDR(p) ((u32)(uintptr_t)(AS3 char*)(p))
#define WAIT_L0() asm volatile("s_waitcnt lgkmcnt(0)" ::: "memory")
#define SGB(m, n) __builtin_amdgcn_sched_group_barrier(m, n, 0)
#define BAR() do { WAIT_L0(); __builtin_amdgcn_s_barrier(); asm volatile("" ::: "memory"); } while (0)

#define AUX0 16
#define AUX1 16
#define AUXT 16
#define TILE_POS 1
#define TILE_LDS 1
#define AUXO 0
#define AUX2 16
#define AUX3 0
template <int AUX> DEVI void st128(srd_t r, const char* base, const char* p, u32x4 v) { __builtin_amdgcn_raw_buffer_store_b128(v, r, (u32)(p - base), 0, AUX); }
template <int AUX> DEVI void st64(srd_t r, const char* base, const char* p, u32x2 v) { __builtin_amdgcn_raw_buffer_store_b64(v, r, (u32)(p - base), 0, AUX); }
template <int AUX> DEVI void st32(srd_t r, const char* base, const char* p, u32 v) { __builtin_amdgcn_raw_buffer_store_b32(v, r, (u32)(p - base), 0, AUX); }

constexpr int S_ = 2048, D_ = 576, FH = 2304, NZQ = 4032;
constexpr size_t OFF_Z1 = 0;
constexpr size_t OFF_ZQ = OFF_Z1 + (size_t)288 * 2048 * 16;
constexpr size_t OFF_ZN = OFF_ZQ + (size_t)256 * NZQ * 16;
constexpr size_t OFF_PO = OFF_ZN + (size_t)256 * FH * 16;
constexpr size_t OFF_WT = OFF_PO + (size_t)360 * 2048 * 16;
constexpr size_t WS_END = OFF_WT + (size_t)360 * 576 * 16;

#define STAMP(ws, k, i)
DEVI float bf_lo(u32 v) { return __builtin_bit_cast(float, v << 16); }
DEVI float bf_hi(u32 v) { return __builtin_bit_cast(float, v & 0xffff0000u); }
DEVI u32 pk_bf16(float a, float b) {
  f32x2 v = {a, b};
  return __builtin_bit_cast(u32, __builtin_convertvector(v, bf16x2));
}
DEVI f32x4 mfma16(u32x4 a, u32x4 b, f32x4 c) {
  return __builtin_amdgcn_mfma_f32_16x16x32_bf16(__builtin_bit_cast(bf16x8, a), __builtin_bit_cast(bf16x8, b), c, 0, 0, 0);
}
DEVI f32x16 mfma32(u32x4 a, u32x4 b, f32x16 c) {
  return __builtin_amdgcn_mfma_f32_32x32x16_bf16(__builtin_bit_cast(bf16x8, a), __builtin_bit_cast(bf16x8, b), c, 0, 0, 0);
}
DEVI u32x2 tr_read(const char* p) {
  s16x4 v = __builtin_amdgcn_ds_read_tr16_b64_v4i16((AS3 s16x4*)p);
  return __builtin_bit_cast(u32x2, v);
}
DEVI u32x4 tr_read2(const char* p0, const char* p1) {
  u32x2 a = tr_read(p0), b = tr_read(p1);
  u32x4 r = {a[0], a[1], b[0], b[1]};
  return r;
}
DEVI float fexp2(float x) { return __builtin_amdgcn_exp2f(x); }
DEVI float frcp(float x) { return __builtin_amdgcn_rcpf(x); }
DEVI float gelu_tanh(float v) {
  const float c0 = -2.0f * 0.7978845608028654f * 1.4426950408889634f;
  const float c1 = c0 * 0.044715f;
  float a = __builtin_fmaf(v * v, c1, c0);
  float e = fexp2(v * a);
  return v * frcp(1.0f + e);
}
DEVI float silu_f(float v) { return v * frcp(1.0f + fexp2(v * -1.4426950408889634f)); }
DEVI int swap23(int i) { return (i & ~12) | ((i & 4) << 1) | ((i & 8) >> 1); }

#define K0_G0 1
constexpr int K0_SLOT = 24576;
#define MAKE_SRDN(p, n) __builtin_amdgcn_make_buffer_rsrc((void*)(p), (short)0, (int)(n), 0x00020000)
template <int V> using IC = std::integral_constant<int, V>;
DEVI void bufl(u32 m0v, u32 voff, srd_t srd, u32 soff) { BUFL(m0v, voff, srd, soff); }
#define CODE_PF 3
#define CODE_WARM(SYM, NB, LEN, LDSDUMP)                                                                             \
  do {                                                                                                           \
    if (CODE_PF && (blockIdx.x >> 3) < (NB) && __builtin_amdgcn_readfirstlane(threadIdx.x >> 6) == 7) {          \
      unsigned long long pc_; int off_;                                                                          \
      asm volatile("s_getpc_b64 %0\n.Lcw%=:\n\ts_mov_b32 %1, " SYM "-.Lcw%=" : "=s"(pc_), "=s"(off_));           \
      const char* code_ = (const char*)(pc_ + (long long)off_);                                                  \
      bufl((LDSDUMP), (blockIdx.x >> 3) * 8192u + (threadIdx.x & 63) * 128u, MAKE_SRDN(code_, (LEN)), 0u);      \
    }                                                                                                            \
  } while (0)
DEVI float dpp_xor1(float v) { return __builtin_bit_cast(float, __builtin_amdgcn_update_dpp(0, __builtin_bit_cast(int, v), 0xB1, 0xf, 0xf, true)); }
DEVI float dpp_xor2(float v) { return __builtin_bit_cast(float, __builtin_amdgcn_update_dpp(0, __builtin_bit_cast(int, v), 0x4E, 0xf, 0xf, true)); }

template <int ZORI, int GELU, int NP7>
DEVI void k0_body(char* lds, const u16* __restrict__ x, const u16* __restrict__ wqkv, const u16* __restrict__ w_in,
                  const u16* __restrict__ w_sp, const u16* __restrict__ w_out, const u16* __restrict__ wo,
                  char* __restrict__ ws, int rt, int cg) {
  const int tid = threadIdx.x, lane = tid & 63;
  const int w = __builtin_amdgcn_readfirstlane(tid >> 6);
  const int tgp = w & 3, cs = w >> 2;
  const int h = lane >> 5;
  const int t0 = rt * 128;
  const int tok = swap23(lane & 31);
  constexpr int NPAIR = 6 + NP7;
  constexpr bool IS_Q = !GELU;
  STAMP(ws, 0, 0);

  u32x4 xr[18];
  const int xrow = 16 * w + (lane >> 2), xj = lane & 3;
  {
    const char* xp = (const char*)x + (size_t)(t0 + xrow) * 1152 + xj * 16;
#pragma unroll
    for (int i = 0; i < 18; ++i) xr[i] = *(const u32x4*)(xp + i * 64);
  }
  constexpr int WSTR = IS_Q ? 3456 : 9216;
  const int wrow = 24 * w + (lane >> 3);
  const int wch = (lane & 7) ^ (((lane >> 4) & 1) << 2);
  const srd_t srdW = MAKE_SRD(IS_Q ? (const char*)wqkv + (cg - 12) * 768 : (const char*)w_in + cg * 768);
  const u32 voffW = wrow * WSTR + wch * 16;
  const int single = 48 + (cg - 6);
  const srd_t srdS = MAKE_SRD((const char*)wqkv + (NP7 ? (single >> 1) * 128 : 0));
  const u32 voffS = wrow * 3456 + wch * 16;
  const u32 ldsw = LDS_ADDR(lds) + w * 3072;
  const srd_t srdP0 = MAKE_SRD((const char*)w_sp + (size_t)blockIdx.x * 32768);
  const srd_t srdZ = MAKE_SRDN(x, 0);
  const srd_t srdO = MAKE_SRD(ws);
  const u32 pidx = blockIdx.x * 8 + w;
  const u32 vl16 = lane * 16;
  auto issue = [&](auto PQ_, auto TH_, auto SL_, auto N_) {
    constexpr int pq = PQ_.value, TH = TH_.value, SL = SL_.value, N = N_.value;
    const u32 m0v = ldsw + SL * K0_SLOT + N * 1024;
    if constexpr (pq < 6) {
      bufl(m0v, voffW, srdW, (u32)(pq * 128 + (TH * 192 + 8 * N) * WSTR));
    } else if constexpr (NP7 && pq == 6) {
      bufl(m0v, voffS, srdS, (u32)((TH * 192 + 8 * N) * 3456));
    } else {
      bufl(m0v, vl16, srdZ, 0u);
    }
  };
  auto pf = [&](auto I_) {
    constexpr int i = I_.value;
    const u32 m0v = LDS_ADDR(lds) + 6 * K0_SLOT + w * 1024;
    if constexpr (i < 4) {
      bufl(m0v, vl16, srdP0, (w * 4 + i) * 1024);
    } else if constexpr (i < 6) {
      const u32 c = pidx * 2 + (i - 4);
      const bool ok = c < 2592;
      bufl(m0v, vl16, MAKE_SRDN(w_out, ok ? FH * D_ * 2 : 0), ok ? c * 1024 : 0u);
    } else if constexpr (i == 6) {
      const bool ok = pidx < 648;
      bufl(m0v, vl16, MAKE_SRDN(wo, ok ? D_ * D_ * 2 : 0), ok ? pidx * 1024 : 0u);
    }
  };
  auto issue3 = [&](auto pq, auto TH_, auto SL_) { issue(pq, TH_, SL_, IC<0>{}); issue(pq, TH_, SL_, IC<1>{}); issue(pq, TH_, SL_, IC<2>{}); };

  u32x4 xa[36];
  {
    float s1 = 0.f, s2 = 0.f;
    const bf16x2 ones = __builtin_bit_cast(bf16x2, 0x3f803f80u);
#pragma unroll
    for (int i = 0; i < 18; ++i) {
#pragma unroll
      for (int j = 0; j < 4; ++j) {
        const u32 xv = xr[i][j];
        bf16x2 v = __builtin_bit_cast(bf16x2, xv);
        s1 = __builtin_amdgcn_fdot2_f32_bf16(v, ones, s1, false);
        s2 = __builtin_amdgcn_fdot2_f32_bf16(v, v, s2, false);
      }
    }
    s1 += dpp_xor1(s1); s2 += dpp_xor1(s2);
    s1 += dpp_xor2(s1); s2 += dpp_xor2(s2);
    const float mean = s1 * (1.0f / 576.0f);
    const float var = fmaxf(s2 * (1.0f / 576.0f) - mean * mean, 0.f);
    const float rstd = __builtin_amdgcn_rsqf(var + 1e-5f);
    const float nb = -mean * rstd;
    const f32x2 r2 = {rstd, rstd}, n2 = {nb, nb};
    STAMP(ws, 0, 1);
    auto norm = [&](int i) {
      f32x2 f[4];
#pragma unroll
      for (int j = 0; j < 4; ++j) { const u32 v = xr[i][j]; f[j][0] = bf_lo(v); f[j][1] = bf_hi(v); }
#pragma unroll
      for (int j = 0; j < 4; ++j) f[j] = __builtin_elementwise_fma(f[j], r2, n2);
      u32x4 o;
#pragma unroll
      for (int j = 0; j < 4; ++j) o[j] = pk_bf16(f[j][0], f[j][1]);
      return o;
    };
    const int fw = (xrow >> 1) & 7;
    char* wA = lds + xrow * 384 + ((xj ^ fw) * 16);
    char* wB = lds + xrow * 384 + (((4 + xj) ^ fw) * 16);
    SB();
    auto nw = [&](auto I_) {
      constexpr int I = I_.value, th = I / 6, m = I % 6;
      *(u32x4*)(((m & 1) ? wB : wA) + th * 49152 + (m >> 1) * 128) = norm(I);
      if constexpr (I & 1) issue(IC<0>{}, IC<(I / 6)>{}, IC<4 + I / 6>{}, IC<(I >> 1) % 3>{});
      SB();
    };
    nw(IC<0>{}); nw(IC<1>{}); nw(IC<2>{}); nw(IC<3>{}); nw(IC<4>{}); nw(IC<5>{});
    nw(IC<6>{}); nw(IC<7>{}); nw(IC<8>{}); nw(IC<9>{}); nw(IC<10>{}); nw(IC<11>{});
    const int row = 32 * tgp + tok;
    const int f = (row >> 1) & 7;
    const char* rb = lds + row * 384;
#define RDX(TH, BASE)                                                                    \
    _Pragma("unroll") for (int s = 0; s < 12; ++s) {                                     \
      int c = 2 * s + h;                                                                 \
      int cp = (c & ~7) | ((c & 7) ^ f);                                                 \
      xa[TH * 12 + s] = *(const u32x4*)(rb + BASE + cp * 16);                            \
    }
    STAMP(ws, 0, 2);
    BAR();
    RDX(0, 0)
    RDX(1, 49152)
    SB();
#pragma unroll
    for (int i = 12; i < 18; ++i) xr[i] = norm(i);
    SB();
    BAR();
    STAMP(ws, 0, 3);
#pragma unroll
    for (int i = 12; i < 18; ++i) {
      const int m = i - 12;
      *(u32x4*)(((m & 1) ? wB : wA) + 49152 + (m >> 1) * 128) = xr[i];
    }
    BAR();
    RDX(2, 49152)
    SB();
    issue3(IC<0>{}, IC<2>{}, IC<0>{});
    issue3(IC<1>{}, IC<0>{}, IC<1>{});
    SB();
    WAIT_L0();
#undef RDX
  }
  STAMP(ws, 0, 4);

  const int q4 = (lane & 15) >> 2, p4 = lane & 3, g16 = (lane >> 4) & 1;
  const int pp = ZORI ? (((p4 & 1) << 1) | (p4 >> 1)) : p4;
  const int rd_off = (8 * h + q4) * 128 + (((4 * cs + 2 * g16 + (pp >> 1)) ^ ((q4 >> 1) << 2)) * 16) + (pp & 1) * 8;
  u32 rdb0 = LDS_ADDR(lds) + rd_off, rdb1 = rdb0 + 2 * K0_SLOT, rdb2 = rdb0 + 4 * K0_SLOT;
  asm volatile("" : "+v"(rdb0), "+v"(rdb1), "+v"(rdb2));
  char* dst0;
  size_t ustep;
  constexpr size_t PSTEP = ZORI ? (size_t)8 * 2048 * 16 : (size_t)64 * 16;
  if (ZORI) {
    dst0 = ws + OFF_Z1 + ((size_t)((cg * 384 + cs * 32) / 8 + h) * 2048 + t0 + 32 * tgp + tok) * 16;
    ustep = (size_t)2 * 2048 * 16;
  } else {
    const int col = (IS_Q ? FH + (cg - 12) * 384 : (cg - 6) * 384) + cs * 32 + (lane & 31);
    dst0 = ws + OFF_ZQ + ((size_t)(t0 / 8 + 4 * tgp + h) * NZQ + col) * 16;
    ustep = (size_t)2 * NZQ * 16;
  }
  u32x4 wf[4];
  auto rdf = [&](int slot, int s) {
    const u32 a = (slot < 2 ? rdb0 : slot < 4 ? rdb1 : rdb2) + (slot & 1) * K0_SLOT + s * 2048;
    const u32x2 lo = __builtin_bit_cast(u32x2, __builtin_amdgcn_ds_read_tr16_b64_v4i16((AS3 s16x4*)(uintptr_t)a));
    const u32x2 hi = __builtin_bit_cast(u32x2, __builtin_amdgcn_ds_read_tr16_b64_v4i16((AS3 s16x4*)(uintptr_t)(a + 512)));
    const u32x4 r = {lo[0], lo[1], hi[0], hi[1]};
    return r;
  };
  f32x2 ee;
  u32x4 eo;
  auto piece = [&](auto K_, const f32x16& pv, char* dst) {
    constexpr int K = K_.value, J = K >> 2, SUB = K & 3;
    const f32x2 v = {pv[2 * J], pv[2 * J + 1]};
    if constexpr (SUB == 0) {
      if (GELU) {
        const float c0 = -2.0f * 0.7978845608028654f * 1.4426950408889634f;
        const float c1 = c0 * 0.044715f;
        const f32x2 c0v = {c0, c0}, c1v = {c1, c1};
        ee = v * __builtin_elementwise_fma(v * v, c1v, c0v);
      }
    } else if constexpr (SUB == 1) {
      if (GELU) { ee[0] = fexp2(ee[0]); ee[1] = fexp2(ee[1]); }
    } else if constexpr (SUB == 2) {
      if (GELU) {
        const f32x2 one = {1.f, 1.f};
        const f32x2 d = ee + one;
        ee[0] = frcp(d[0]); ee[1] = frcp(d[1]);
      }
    } else {
      if (GELU) {
        const f32x2 o = v * ee;
        eo[J & 3] = pk_bf16(o[0], o[1]);
      } else {
        eo[J & 3] = pk_bf16(v[0], v[1]);
      }
      if ((J & 3) == 3) st128<AUX0>(srdO, ws, dst + (J >> 2) * ustep, eo);
    }
  };
  auto pair = [&](auto P_, auto SPL_, f32x16& acc, const f32x16& pv) {
    constexpr int p = P_.value, ODD = p & 1, PREV = p > 0, SPL = SPL_.value;
    constexpr int NS = SPL ? 6 : 12, S0 = SPL ? 6 * (SPL - 1) : 0;
    char* dst = dst0 + (size_t)(p - 1) * PSTEP;
#pragma unroll
    for (int i = 0; i < 16; ++i) acc[i] = 0.f;
    auto stage = [&](auto TH_) {
      constexpr int TH = TH_.value;
      constexpr int SL = (3 * ODD + TH + 4) % 6, SN = (SL + 1) % 6, SI = (SL + 5) % 6;
      constexpr int ITH = (TH + 2) % 3;
      constexpr bool FIRST = !PREV && TH == 0;
      constexpr int ip = p + (TH == 0 ? 1 : 2);
      if (FIRST || (SPL && TH == 1)) WAIT_V(6); else if (!(SPL && TH == 2)) WAIT_V(9);
      __builtin_amdgcn_s_barrier();
      SB();
      if ((!PREV || SPL) && TH == 0) { wf[0] = rdf(SL, S0); wf[1] = rdf(SL, S0 + 1); wf[2] = rdf(SL, S0 + 2); SB(); }
      auto step = [&](auto J_) {
        constexpr int J = J_.value, S = S0 + J, G = TH * NS + J;
        if constexpr (J < NS) {
          if (ZORI) acc = mfma32(wf[G & 3], xa[TH * 12 + S], acc);
          else      acc = mfma32(xa[TH * 12 + S], wf[G & 3], acc);
          SB();
          if (!(SPL && TH == 2 && J + 3 >= NS)) wf[(G + 3) & 3] = (J + 3 < NS) ? rdf(SL, S + 3) : rdf(SN, S0 + J + 3 - NS);
          if constexpr (!SPL) {
            if constexpr (FIRST && (S == 0 || S == 2 || S == 4)) issue(IC<1>{}, IC<1>{}, IC<2>{}, IC<S / 2>{});
            if constexpr (S == (FIRST ? 6 : 1)) issue(IC<ip>{}, IC<ITH>{}, IC<SI>{}, IC<0>{});
            if constexpr (S == (FIRST ? 8 : 5)) issue(IC<ip>{}, IC<ITH>{}, IC<SI>{}, IC<1>{});
            if constexpr (S == (FIRST ? 10 : 9)) issue(IC<ip>{}, IC<ITH>{}, IC<SI>{}, IC<2>{});
            if constexpr (S == 11 && PREV) pf(IC<(p - 1) * 3 + TH>{});
          }
          if constexpr (PREV && !SPL && G >= K0_G0 && G < 32 + K0_G0) piece(IC<G - K0_G0>{}, pv, dst);
          if constexpr (PREV && SPL && G < 16) { piece(IC<2 * G>{}, pv, dst); piece(IC<2 * G + 1>{}, pv, dst); }
          SB();
        }
      };
      step(IC<0>{}); step(IC<1>{}); step(IC<2>{}); step(IC<3>{}); step(IC<4>{}); step(IC<5>{});
      step(IC<6>{}); step(IC<7>{}); step(IC<8>{}); step(IC<9>{}); step(IC<10>{}); step(IC<11>{});
    };
    stage(IC<0>{}); stage(IC<1>{}); stage(IC<2>{});
  };
  f32x16 accA, accB;
  pair(IC<0>{}, IC<0>{}, accA, accA);
  pair(IC<1>{}, IC<0>{}, accB, accA);
  pair(IC<2>{}, IC<0>{}, accA, accB);
  pair(IC<3>{}, IC<0>{}, accB, accA);
  pair(IC<4>{}, IC<0>{}, accA, accB);
  pair(IC<5>{}, IC<0>{}, accB, accA);
  STAMP(ws, 0, 5);
  if (NP7) {
    if ((single & 1) != cs) { rdb0 ^= 64; rdb1 ^= 64; rdb2 ^= 64; }
    char* xch = lds + 4 * K0_SLOT + lane * 16;
    char* dst = ws + OFF_ZQ + ((size_t)(t0 / 8 + 4 * tgp + h) * NZQ + FH + single * 32 + (lane & 31)) * 16;
    auto tail = [&](auto CS_) {
      constexpr int CS = CS_.value;
      pair(IC<6>{}, IC<1 + CS>{}, accA, accB);
      f32x4 s0, s1;
#pragma unroll
      for (int j = 0; j < 4; ++j) { s0[j] = accA[8 * (1 - CS) + j]; s1[j] = accA[8 * (1 - CS) + 4 + j]; }
      *(f32x4*)(xch + w * 2048) = s0;
      *(f32x4*)(xch + w * 2048 + 1024) = s1;
      BAR();
      s0 = *(const f32x4*)(xch + (w ^ 4) * 2048);
      s1 = *(const f32x4*)(xch + (w ^ 4) * 2048 + 1024);
      u32x4 o;
      o[0] = pk_bf16(accA[8 * CS + 0] + s0[0], accA[8 * CS + 1] + s0[1]);
      o[1] = pk_bf16(accA[8 * CS + 2] + s0[2], accA[8 * CS + 3] + s0[3]);
      o[2] = pk_bf16(accA[8 * CS + 4] + s1[0], accA[8 * CS + 5] + s1[1]);
      o[3] = pk_bf16(accA[8 * CS + 6] + s1[2], accA[8 * CS + 7] + s1[3]);
      st128<AUX0>(srdO, ws, dst + CS * ustep, o);
    };
    if (cs == 0) tail(IC<0>{}); else tail(IC<1>{});
  } else {
    char* dst = dst0 + 5 * PSTEP;
#pragma unroll
    for (int u = 0; u < 2; ++u) {
      u32x4 o;
#pragma unroll
      for (int j = 0; j < 4; ++j)
        o[j] = GELU ? pk_bf16(gelu_tanh(accB[8 * u + 2 * j]), gelu_tanh(accB[8 * u + 2 * j + 1])) : pk_bf16(accB[8 * u + 2 * j], accB[8 * u + 2 * j + 1]);
      st128<AUX0>(srdO, ws, dst + u * ustep, o);
    }
  }
  WAIT_V(0);
  STAMP(ws, 0, 6);
}

__global__ __launch_bounds__(512, 2) void k0_inproj(const u16* __restrict__ x, const u16* __restrict__ wqkv,
                                                    const u16* __restrict__ w_in, const u16* __restrict__ w_sp,
                                                    const u16* __restrict__ w_out, const u16* __restrict__ wo, char* __restrict__ ws) {
  __shared__ __attribute__((aligned(16))) char lds[6 * K0_SLOT + 8192];
  const int b = blockIdx.x, xcd = b & 7, idx = b >> 3;
  const int cg = xcd * 2 + (idx & 1), rt = idx >> 1;
  CODE_WARM("_Z9k0_inprojPKtS0_S0_S0_S0_S0_Pc", 6, 48640, LDS_ADDR(lds) + 6 * K0_SLOT + 7 * 1024);
  CODE_WARM("_Z6k1_midPKtS0_Pc", 2, 9216, LDS_ADDR(lds) + 6 * K0_SLOT + 7 * 1024);
  if (cg < 6)       k0_body<1, 1, 0>(lds, x, wqkv, w_in, w_sp, w_out, wo, ws, rt, cg);
  else if (cg < 12) k0_body<0, 1, 1>(lds, x, wqkv, w_in, w_sp, w_out, wo, ws, rt, cg);
  else              k0_body<0, 0, 0>(lds, x, wqkv, w_in, w_sp, w_out, wo, ws, rt, cg);
}

constexpr int AT_TG = 320;
constexpr int AT_K = 0, AT_V = 64 * AT_TG, AT_Q = 2 * 64 * AT_TG, AT_ONES = AT_Q + 32 * AT_TG;

constexpr int AT_TR = ((AT_ONES + 16 + 512 + 127) / 128) * 128;
constexpr int AT_RED = AT_ONES + 16;
template <int CTRL> DEVI float dpp_mov(float v) {
  return __builtin_bit_cast(float, __builtin_amdgcn_update_dpp(0, __builtin_bit_cast(int, v), CTRL, 0xf, 0xf, true));
}
DEVI float sum32(float a, float b) {
  auto r = __builtin_amdgcn_permlane32_swap(__builtin_bit_cast(u32, a), __builtin_bit_cast(u32, b), false, false);
  u32 r0 = r[0], r1 = r[1];
  return __builtin_bit_cast(float, r0) + __builtin_bit_cast(float, r1);
}
DEVI float sum16(float a, float b) {
  auto r = __builtin_amdgcn_permlane16_swap(__builtin_bit_cast(u32, a), __builtin_bit_cast(u32, b), false, false);
  u32 r0 = r[0], r1 = r[1];
  return __builtin_bit_cast(float, r0) + __builtin_bit_cast(float, r1);
}

__global__ __launch_bounds__(512, 2) void k1_mid(const u16* __restrict__ w_out, const u16* __restrict__ wo, char* __restrict__ ws) {
  __shared__ __attribute__((aligned(16))) char lds[AT_TR + 16384];
  const int b = blockIdx.x;
  const int head = (b & 7) * 4 + ((b >> 3) & 3), qb = b >> 5;
  const int tid = threadIdx.x, lane = tid & 63;
  const int w = __builtin_amdgcn_readfirstlane(tid >> 6);
  const int h = lane >> 5;
  STAMP(ws, 1, 0);
  const srd_t srdO = MAKE_SRD(ws);
  CODE_WARM("_Z6k1_midPKtS0_Pc", 2, 9216, LDS_ADDR(lds) + AT_TR);
  CODE_WARM("_Z10k2_spatialPKtS0_Pc", 2, 11520, LDS_ADDR(lds) + AT_TR);
  const int tl = tid - 256, g = b * 102 + tl;
  const bool tile_on = (tl >= 0) && (tl < 102) && (g < 360 * 72);
  const int fc = g / 72, dq = g - fc * 72;
  u32x4 tr[8];
#pragma unroll
  for (int i = 0; i < 8; ++i) tr[i] = u32x4{0u, 0u, 0u, 0u};
  if (tile_on) {
    const char* src = (fc < 288) ? (const char*)w_out + (size_t)fc * 8 * 1152 : (const char*)wo + (size_t)(fc - 288) * 8 * 1152;
    src += dq * 16;
#pragma unroll
    for (int i = 0; i < 8; ++i) tr[i] = *(const u32x4*)(src + i * 1152);
  }
  auto tile_store = [&]() {
    if (w == 4 || w == 5) {
      char* tl_ = lds + AT_TR + (w - 4) * 8192;
#pragma unroll
      for (int d = 0; d < 8; ++d) {
        u32x4 o;
#pragma unroll
        for (int m = 0; m < 4; ++m)
          o[m] = __builtin_amdgcn_perm(tr[2 * m + 1][d >> 1], tr[2 * m][d >> 1], (d & 1) ? 0x07060302u : 0x05040100u);
        *(u32x4*)(tl_ + lane * 128 + ((d ^ (lane & 7)) * 16)) = o;
      }
      const int g0 = b * 102 + (w - 4) * 64;
      int nv = 102 - (w - 4) * 64;
      nv = nv > 64 ? 64 : nv;
      nv = nv < 360 * 72 - g0 ? nv : 360 * 72 - g0;
      nv = __builtin_amdgcn_readfirstlane(nv < 0 ? 0 : nv);
      char* dst0 = ws + OFF_WT + (size_t)__builtin_amdgcn_readfirstlane(g0) * 128;
      const srd_t srdT = MAKE_SRDN(dst0, nv * 128);
#pragma unroll
      for (int d = 0; d < 8; ++d) {
        const int row = d * 8 + (lane >> 3);
        const u32x4 o = *(const u32x4*)(tl_ + row * 128 + (((lane & 7) ^ (row & 7)) * 16));
        st128<AUXT>(srdT, dst0, dst0 + d * 1024 + lane * 16, o);
      }
    }
  };
  u32x4 zv[5];
  {
    const srd_t srz = MAKE_SRDN(ws + OFF_ZQ + (size_t)b * NZQ * 16, FH * 16);
    const u32 vo = (u32)tid * 16u;
    asm volatile("buffer_load_dwordx4 %0, %5, %6, 0 offen\n\t"
                 "buffer_load_dwordx4 %1, %5, %6, %7 offen\n\t"
                 "buffer_load_dwordx4 %2, %5, %6, %8 offen\n\t"
                 "buffer_load_dwordx4 %3, %5, %6, %9 offen\n\t"
                 "buffer_load_dwordx4 %4, %10, %6, 0 offen"
                 : "=&v"(zv[0]), "=&v"(zv[1]), "=&v"(zv[2]), "=&v"(zv[3]), "=&v"(zv[4])
                 : "v"(vo), "s"(srz), "s"(8192u), "s"(16384u), "s"(24576u), "v"(vo + 32768u)
                 : "memory");
  }
  {
    const srd_t srd = MAKE_SRD(ws + OFF_ZQ);
    const u32 l0 = LDS_ADDR(lds);
    const int tgk0 = 32 * qb - 32;
#pragma unroll
    for (int it = 0; it < 7; ++it) {
      const int n = w + 8 * it;
      if (it < 6 || w < 2) {
        const int part = n < 20 ? 1 : n < 40 ? 2 : 0;
        const int nn = n < 20 ? n : n < 40 ? n - 20 : n - 40;
        const int i = nn * 64 + lane;
        const int tg = i / 20;
        int d = i - tg * 20;
        d = d > 17 ? 17 : d;
        int gtg = (part == 0 ? 32 * qb : tgk0) + tg;
        gtg = gtg < 0 ? 0 : gtg;
        const u32 voff = (u32)(gtg * NZQ + d) * 16u;
        const u32 soff = (u32)(FH + 576 * part + 18 * head) * 16u;
        bufl(l0 + (part == 1 ? AT_K : part == 2 ? AT_V : AT_Q) + nn * 1024, voff, srd, soff);
      }
    }
  }
  if (tid == 0) { u32x4 o = {0x3f803f80u, 0x3f803f80u, 0x3f803f80u, 0x3f803f80u}; *(u32x4*)(lds + AT_ONES) = o; }
  asm volatile("s_waitcnt vmcnt(6)" : "+v"(zv[0]), "+v"(zv[1]), "+v"(zv[2]), "+v"(zv[3]), "+v"(zv[4])::"memory");
  f32x2 zx[5][4];
  {
    f32x2 S[4], Q[4];
#pragma unroll
    for (int e = 0; e < 4; ++e) {
#pragma unroll
      for (int i = 0; i < 5; ++i) {
        const u32 u = zv[i][e];
        zx[i][e] = f32x2{bf_lo(u), bf_hi(u)};
        if (i == 0) { S[e] = zx[0][e]; Q[e] = zx[0][e] * zx[0][e]; }
        else { S[e] += zx[i][e]; Q[e] = __builtin_elementwise_fma(zx[i][e], zx[i][e], Q[e]); }
      }
    }
    float r8[8], u4[4];
#pragma unroll
    for (int e = 0; e < 8; ++e) { const float a = S[e >> 1][e & 1], q = Q[e >> 1][e & 1]; r8[e] = sum32(a, q); }
#pragma unroll
    for (int e = 0; e < 4; ++e) {
      float u = sum16(r8[e], r8[e + 4]);
      u += dpp_mov<0x128>(u); u += dpp_mov<0x124>(u); u += dpp_mov<0x122>(u); u += dpp_mov<0x121>(u);
      u4[e] = u;
    }
    if ((lane & 15) == 0) *(f32x4*)(lds + AT_RED + w * 64 + (lane >> 4) * 16) = f32x4{u4[0], u4[1], u4[2], u4[3]};
  }
  __builtin_amdgcn_s_waitcnt(0x0F70);
  WAIT_V(0);
  BAR();
  STAMP(ws, 1, 1);
  if (TILE_POS == 0) tile_store();
  {
    const float* red = (const float*)(lds + AT_RED) + (lane & 15);
    float tot = 0.f;
#pragma unroll
    for (int i = 0; i < 8; ++i) tot += red[16 * i];
    const float q = dpp_mov<0x128>(tot);
    const float mean = tot * (1.0f / 2304.0f);
    const float var = fmaxf(__builtin_fmaf(-mean, mean, q * (1.0f / 2304.0f)), 0.f);
    const float rs = __builtin_amdgcn_rsqf(var + 1e-5f), nb = -mean * rs;
    f32x2 rs2[4], nb2[4];
    rs2[0] = f32x2{dpp_mov<0x150>(rs), dpp_mov<0x151>(rs)}; nb2[0] = f32x2{dpp_mov<0x150>(nb), dpp_mov<0x151>(nb)};
    rs2[1] = f32x2{dpp_mov<0x152>(rs), dpp_mov<0x153>(rs)}; nb2[1] = f32x2{dpp_mov<0x152>(nb), dpp_mov<0x153>(nb)};
    rs2[2] = f32x2{dpp_mov<0x154>(rs), dpp_mov<0x155>(rs)}; nb2[2] = f32x2{dpp_mov<0x154>(nb), dpp_mov<0x155>(nb)};
    rs2[3] = f32x2{dpp_mov<0x156>(rs), dpp_mov<0x157>(rs)}; nb2[3] = f32x2{dpp_mov<0x156>(nb), dpp_mov<0x157>(nb)};
    char* dst = ws + OFF_ZN + ((size_t)b * FH + tid) * 16;
#pragma unroll
    for (int i = 0; i < 5; ++i) {
      u32x4 o;
#pragma unroll
      for (int e = 0; e < 4; ++e) {
        const f32x2 y = __builtin_elementwise_fma(zx[i][e], rs2[e], nb2[e]);
        o[e] = pk_bf16(y[0], y[1]);
      }
      if (i < 4 || w < 4) st128<AUX1>(srdO, ws, dst + i * 8192, o);
    }
  }
  if (TILE_POS == 1) tile_store();
  SB();
  STAMP(ws, 1, 2);
  const int q4 = (lane & 15) >> 2, p4 = lane & 3, g16 = (lane >> 4) & 1;
  u32x4 qf0, qf1;
  {
    const char* qb_ = lds + AT_Q + (4 * w + 2 * g16 + (p4 >> 1)) * AT_TG + (p4 & 1) * 8;
    int d0 = 8 * h + q4;
    qf0 = tr_read2(qb_ + d0 * 16, qb_ + (d0 + 4) * 16);
    int d1 = 16 + q4; d1 = d1 > 17 ? 17 : d1;
    u32x2 t = tr_read(qb_ + d1 * 16);
    qf1[0] = h ? 0u : t[0]; qf1[1] = 0u; qf1[2] = 0u; qf1[3] = 0u;
  }
  const char* kb0 = lds + AT_K + (4 * w + 2 * g16 + (p4 & 1)) * AT_TG + (p4 >> 1) * 8;
  const int kd0 = (8 * h + q4) * 16;
  const int kd1 = (16 + q4 > 17 ? 17 : 16 + q4) * 16;
  const int n = lane & 31;
  const float NEG = -3.0e38f;
  f32x16 s[9];
#pragma unroll
  for (int jj = 0; jj < 9; ++jj) {
    const char* kb = kb0 + jj * 4 * AT_TG;
    u32x4 k0 = tr_read2(kb + kd0, kb + kd0 + 64);
    u32x2 k1a = tr_read(kb + kd1);
    u32x4 k1 = {k1a[0], k1a[1], k1a[0], k1a[1]};
    f32x16 z;
#pragma unroll
    for (int i = 0; i < 16; ++i) z[i] = 0.f;
    s[jj] = mfma32(k1, qf1, mfma32(k0, qf0, z));
  }
  if (qb == 0) {
#pragma unroll
    for (int jj = 0; jj < 8; ++jj)
      if (jj < 8 - w) {
        asm volatile("" ::: "memory");
#pragma unroll
        for (int r = 0; r < 16; ++r) s[jj][r] = NEG;
      }
  }
  u32 mk[8];
  {
    const int nb = n - 8 * h;
#pragma unroll
    for (int j = 0; j < 8; ++j) {
      const int k0 = ((2 * j) & 7) + 16 * (j >> 2);
      const bool c0 = k0 <= nb, c1 = k0 + 1 <= nb;
      s[0][2 * j] = c0 ? s[8][2 * j] : s[0][2 * j];
      s[0][2 * j + 1] = c1 ? s[8][2 * j + 1] : s[0][2 * j + 1];
      mk[j] = c1 ? 0xffffffffu : (c0 ? 0x0000ffffu : 0u);
    }
  }
  float m = __builtin_fmaxf(s[0][0], s[0][1]);
#pragma unroll
  for (int jj = 0; jj < 8; ++jj)
#pragma unroll
    for (int r = (jj == 0 ? 2 : 0); r < 16; r += 2) m = __builtin_fmaxf(__builtin_fmaxf(m, s[jj][r]), s[jj][r + 1]);
  {
    auto r = __builtin_amdgcn_permlane32_swap(__builtin_bit_cast(u32, m), __builtin_bit_cast(u32, m), false, false);
    u32 r0 = r[0], r1 = r[1];
    m = __builtin_fmaxf(__builtin_bit_cast(float, r0), __builtin_bit_cast(float, r1));
  }
  STAMP(ws, 1, 3);
  const float c = 0.23570226039551584f * 1.4426950408889634f;
  const f32x2 c2 = {c, c}, mc2 = {-m * c, -m * c};
  f32x16 o;
#pragma unroll
  for (int i = 0; i < 16; ++i) o[i] = 0.f;
  const char* vb0 = (n == 18) ? lds + AT_ONES : lds + AT_V + (4 * w + h) * AT_TG + n * 16;
  const int vstep = (n == 18) ? 0 : AT_TG;
  u32x4 va = *(const u32x4*)(vb0 + 4 * vstep), vb = *(const u32x4*)(vb0 + 6 * vstep);
#pragma unroll
  for (int jj = 1; jj <= 8; ++jj) {
    const int tt = jj & 7;
    const int nx = jj < 8 ? jj + 1 : 0;
    const u32x4 van = *(const u32x4*)(vb0 + (4 * nx) * vstep), vbn = *(const u32x4*)(vb0 + (4 * nx + 2) * vstep);
    u32x4 p0, p1;
#pragma unroll
    for (int j = 0; j < 4; ++j) {
      const f32x2 a = __builtin_elementwise_fma(f32x2{s[tt][2 * j], s[tt][2 * j + 1]}, c2, mc2);
      const f32x2 bq = __builtin_elementwise_fma(f32x2{s[tt][8 + 2 * j], s[tt][8 + 2 * j + 1]}, c2, mc2);
      p0[j] = pk_bf16(fexp2(a[0]), fexp2(a[1]));
      p1[j] = pk_bf16(fexp2(bq[0]), fexp2(bq[1]));
    }
    if (jj < 8) {
      o = mfma32(va, p0, o);
      o = mfma32(vb, p1, o);
    } else {
      u32x4 m0, m1;
#pragma unroll
      for (int j = 0; j < 4; ++j) { m0[j] = p0[j] & mk[j]; m1[j] = p1[j] & mk[4 + j]; }
      o = mfma32(va, m0, o);
      o = mfma32(vb, m1, o);
#pragma unroll
      for (int j = 0; j < 4; ++j) { m0[j] ^= p0[j]; m1[j] ^= p1[j]; }
      o = mfma32(van, m0, o);
      o = mfma32(vbn, m1, o);
    }
    va = van; vb = vbn;
    SB();
  }
  STAMP(ws, 1, 4);
  float l = o[10];
  {
    auto r = __builtin_amdgcn_permlane32_swap(__builtin_bit_cast(u32, l), __builtin_bit_cast(u32, l), false, false);
    u32 r0 = r[0];
    l = __builtin_bit_cast(float, r0);
  }
  const float inv = 1.0f / l;
  const int t = 256 * qb + 32 * w + n;
  char* po = ws + OFF_PO + (size_t)t * 16;
  const int cb = 18 * head + 4 * h;
#pragma unroll
  for (int pr = 0; pr < 5; ++pr) {
    const int r = 2 * pr;
    const int d = (r & 3) + 8 * (r >> 2);
    if (pr < 4 || h == 0) {
      int cc = cb + d;
      u32 v = pk_bf16(o[r] * inv, o[r + 1] * inv);
      st32<AUXO>(srdO, ws, po + (size_t)(288 + (cc >> 3)) * 2048 * 16 + (cc & 7) * 2, v);
    }
  }
  if (TILE_POS == 2) tile_store();
  STAMP(ws, 1, 5);
}

constexpr int K2_SLOT = 16384 + 8 * 2304;

template <int EXTRA>
DEVI void k2_body(char* lds, const u16* __restrict__ wsp, const u16* __restrict__ bsp, char* __restrict__ ws) {
  const int tid = threadIdx.x, lane = tid & 63;
  const int w = __builtin_amdgcn_readfirstlane(tid >> 6);
  const int kh = w >> 2, tq = w & 3;
  const int b = blockIdx.x, xcd = b & 7, idx = b >> 3;
  const int tb = (xcd & 3) * 4 + (idx & 3), fb = (xcd >> 2) * 8 + (idx >> 2);
  const int t0 = tb * 128, f0 = fb * 144;
  STAMP(ws, 2, 0);

  u32 wv0, wv1, zv[3];
  {
    int r0 = 16 * w + (lane >> 3), r1 = r0 + 8;
    wv0 = (u32)(r0 * 4096 + (((lane & 7) ^ ((r0 >> 1) & 7)) * 16));
    wv1 = (u32)(r1 * 4096 + (((lane & 7) ^ ((r1 >> 1) & 7)) * 16));
#pragma unroll
    for (int j = 0; j < 3; ++j) {
      int e = (w + 8 * j) * 64 + lane;
      int tg = e / 144, f = e - tg * 144;
      zv[j] = (u32)((tg * FH + f) * 16);
    }
  }
  const srd_t wsrd = MAKE_SRD((const char*)wsp + (size_t)t0 * 4096);
  const srd_t zsrd = MAKE_SRD(ws + OFF_ZN + (size_t)f0 * 16);
  const u32 ldsb = LDS_ADDR(lds) + w * 1024;
  auto piece = [&](int it, int j) {
    const u32 sl = ldsb + (it & 3) * K2_SLOT;
    const u32 wo = it * 128, zo = it * (8 * FH * 16);
    if (j == 0) BUFL(sl + w * 1024, wv0, wsrd, wo);
    if (j == 1) BUFL(sl + 16384, zv[0], zsrd, zo);
    if (j == 2) BUFL(sl + w * 1024 + 1024, wv1, wsrd, wo);
    if (j == 3) BUFL(sl + 16384 + 8192, zv[1], zsrd, zo);
    if (j == 4 && EXTRA) BUFL(sl + 16384 + 16384, zv[2], zsrd, zo);
  };
  auto issue = [&](int it) { piece(it, 0); piece(it, 1); piece(it, 2); piece(it, 3); piece(it, 4); };
#define K2_WAIT(n) do { if (EXTRA) WAIT_V_(n * 5); else WAIT_V_(n * 4); } while (0)
#define K2_WAITZ(n) do { if (EXTRA) WAIT_V_(n * 5 + 12); else WAIT_V_(n * 4 + 12); } while (0)
  issue(0); issue(1); issue(2); issue(3);

  const int g = lane >> 4, l15 = lane & 15;
  f32x4 acc[9][2];
#pragma unroll
  for (int i = 0; i < 9; ++i) { acc[i][0] = f32x4{0.f, 0.f, 0.f, 0.f}; acc[i][1] = f32x4{0.f, 0.f, 0.f, 0.f}; }
  int woff[2];
#pragma unroll
  for (int ti = 0; ti < 2; ++ti) {
    int row = 32 * tq + 16 * ti + l15;
    woff[ti] = row * 128 + (((4 * kh + g) ^ ((row >> 1) & 7)) * 16);
  }
  const int zoff = 16384 + (4 * kh + g) * 2304 + l15 * 16;

  struct Frags { u32x4 a[9]; u32x4 b[2]; };
  auto rdb = [&](int it, Frags& F) {
    const char* sl = lds + (it & 3) * K2_SLOT;
    F.b[0] = *(const u32x4*)(sl + woff[0]);
    F.b[1] = *(const u32x4*)(sl + woff[1]);
  };
  auto rda = [&](int it, Frags& F, int fi) {
    const char* sl = lds + (it & 3) * K2_SLOT;
    F.a[fi] = *(const u32x4*)(sl + zoff + fi * 256);
  };
  auto mm1 = [&](const Frags& F, int fi) {
    acc[fi][0] = mfma16(F.a[fi], F.b[0], acc[fi][0]);
    acc[fi][1] = mfma16(F.a[fi], F.b[1], acc[fi][1]);
  };
  auto stage = [&](const Frags& C, Frags& N, int rit, int lit, auto LDc, auto RDc) {
    constexpr bool LD = decltype(LDc)::value, RD = decltype(RDc)::value;
    SB();
    if (RD) rdb(rit, N);
    mm1(C, 0); SB();
    if (RD) { rda(rit, N, 0); rda(rit, N, 1); }
    mm1(C, 1); if (LD) piece(lit, 0); SB();
    if (RD) { rda(rit, N, 2); rda(rit, N, 3); }
    mm1(C, 2); SB();
    if (RD) { rda(rit, N, 4); rda(rit, N, 5); }
    mm1(C, 3); if (LD) piece(lit, 1); SB();
    if (RD) { rda(rit, N, 6); rda(rit, N, 7); }
    mm1(C, 4); SB();
    if (RD) rda(rit, N, 8);
    mm1(C, 5); if (LD) piece(lit, 2); SB();
    mm1(C, 6); SB();
    mm1(C, 7); if (LD) piece(lit, 3); SB();
    mm1(C, 8); if (LD) piece(lit, 4); SB();
  };
  using LDT = std::true_type;
  using T = std::true_type; using Fa = std::false_type;
  Frags F0, F1;
  K2_WAIT(3); BAR();
  STAMP(ws, 2, 1);
  rdb(0, F0);
#pragma unroll
  for (int fi = 0; fi < 9; ++fi) rda(0, F0, fi);
  for (int it = 0; it < 28; it += 2) {
    K2_WAIT(2); BAR();
    stage(F0, F1, it + 1, it + 4, LDT{}, T{});
    K2_WAIT(2); BAR();
    stage(F1, F0, it + 2, it + 5, LDT{}, T{});
  }
  STAMP(ws, 2, 2);
  const size_t eoff = ((size_t)(f0 / 8 + (g >> 1)) * 2048 + t0 + 32 * tq + l15) * 16 + (g & 1) * 8;
  u32x2 z1v[5][2];
  u32 biasr[2];
  K2_WAIT(2); BAR();
#pragma unroll
  for (int fi = 0; fi < 5; ++fi) {
    const int fa = (kh == 0) ? fi : (fi < 4 ? 5 + fi : 8);
#pragma unroll
    for (int ti = 0; ti < 2; ++ti)
      z1v[fi][ti] = *(const u32x2*)(ws + OFF_Z1 + eoff + ((size_t)fa * 2 * 2048 + ti * 16) * 16);
  }
#pragma unroll
  for (int ti = 0; ti < 2; ++ti) biasr[ti] = (u32)bsp[t0 + 32 * tq + 16 * ti + l15];
  stage(F0, F1, 29, 0, Fa{}, T{});
  K2_WAITZ(1); BAR(); stage(F1, F0, 30, 0, Fa{}, T{});
  K2_WAITZ(0); BAR(); stage(F0, F1, 31, 0, Fa{}, T{});
  stage(F1, F0, 0, 0, Fa{}, Fa{});
  STAMP(ws, 2, 3);
  float bias[2] = {bf_lo(biasr[0]), bf_lo(biasr[1])};
  BAR();
  char* xch = lds + tq * 18432 + lane * 16;
  auto finish = [&](auto KH) {
    constexpr int khc = decltype(KH)::value;
    constexpr int lo = khc ? 5 : 0, hi = khc ? 9 : 5;
#pragma unroll
    for (int fi = 0; fi < 9; ++fi)
      if (fi < lo || fi >= hi) {
        *(f32x4*)(xch + (fi * 2 + 0) * 1024) = acc[fi][0];
        *(f32x4*)(xch + (fi * 2 + 1) * 1024) = acc[fi][1];
      }
    BAR();
#pragma unroll
    for (int fi = lo; fi < hi; ++fi)
#pragma unroll
      for (int ti = 0; ti < 2; ++ti) {
        f32x4 o = *(const f32x4*)(xch + (fi * 2 + ti) * 1024);
        f32x4 gq = acc[fi][ti] + o;
        u32x2 z = z1v[fi - lo][ti];
        u32x2 r;
        r[0] = pk_bf16(bf_lo(z[0]) * (gq[0] + bias[ti]), bf_hi(z[0]) * (gq[1] + bias[ti]));
        r[1] = pk_bf16(bf_lo(z[1]) * (gq[2] + bias[ti]), bf_hi(z[1]) * (gq[3] + bias[ti]));
        st64<AUX2>(MAKE_SRD(ws), ws, ws + OFF_PO + eoff + ((size_t)fi * 2 * 2048 + ti * 16) * 16, r);
      }
  };
  if (kh == 0) finish(std::integral_constant<int, 0>{});
  else         finish(std::integral_constant<int, 1>{});
  STAMP(ws, 2, 4);
}

__global__ __launch_bounds__(512, 2) void k2_spatial(const u16* __restrict__ wsp, const u16* __restrict__ bsp, char* __restrict__ ws) {
  __shared__ __attribute__((aligned(16))) char lds[4 * K2_SLOT + 1024];
  CODE_WARM("_Z10k2_spatialPKtS0_Pc", 2, 11520, LDS_ADDR(lds) + 4 * K2_SLOT);
  CODE_WARM("_Z6k3_outPKtPcPt", 2, 13824, LDS_ADDR(lds) + 4 * K2_SLOT);
  if (__builtin_amdgcn_readfirstlane(threadIdx.x >> 6) < 2) k2_body<1>(lds, wsp, bsp, ws);
  else                   k2_body<0>(lds, wsp, bsp, ws);
}

constexpr int K3_SLOT = 8192 + 9216;

template <int EXTRA>
DEVI void k3_body(char* lds, const u16* __restrict__ x, char* __restrict__ ws, u16* __restrict__ out) {
  const int tid = threadIdx.x, lane = tid & 63;
  const int w = __builtin_amdgcn_readfirstlane(tid >> 6);
  const int kh = w >> 2, th = (w >> 1) & 1, dh = (w & 1) ^ kh;
  const int b = blockIdx.x, xcd = b & 7, idx = b >> 3;
  const int tb = xcd * 4 + (idx & 3), db = idx >> 2;
  const int t0 = tb * 64, d0 = db * 72;
  STAMP(ws, 3, 0);

  u32 pv = (u32)((w * 2048 + lane) * 16), wv[2];
#pragma unroll
  for (int j = 0; j < 2; ++j) {
    int e = (w + 8 * j) * 64 + lane;
    int fc = e / 72, dp = e - fc * 72;
    int d = ((fc & 1) && dp < 64) ? (dp ^ 8) : dp;
    wv[j] = (u32)((fc * 576 + d) * 16);
  }
  const srd_t psrd = MAKE_SRD(ws + OFF_PO + (size_t)t0 * 16);
  const srd_t wsrd = MAKE_SRD(ws + OFF_WT + (size_t)d0 * 16);
  const u32 ldsb = LDS_ADDR(lds) + w * 1024;
  auto piece = [&](int it, int j) {
    const u32 sl = ldsb + (it & 7) * K3_SLOT;
    if (j == 0) BUFL(sl, pv, psrd, it * (8 * 2048 * 16));
    if (j == 1) BUFL(sl + 8192, wv[0], wsrd, it * (8 * 576 * 16));
    if (j == 2 && EXTRA) BUFL(sl + 8192 + 8192, wv[1], wsrd, it * (8 * 576 * 16));
  };
  auto issue = [&](int it) { piece(it, 0); piece(it, 1); piece(it, 2); };
#define K3_WAIT(n, z) do { if (EXTRA) WAIT_V_((n) * 3 + (z)); else WAIT_V_((n) * 2 + (z)); } while (0)
#pragma unroll
  for (int i = 0; i < 8; ++i) issue(i);

  const int g = lane >> 4, l15 = lane & 15;
  const int poff = (4 * kh + g) * 1024 + (32 * th + l15) * 16;
  int aoff[3];
#pragma unroll
  for (int di = 0; di < 3; ++di) {
    int d = 48 * dh + 16 * di + l15;
    if (d >= 72) d = 64 + (l15 & 7);
    int dp = ((g & 1) && d < 64) ? (d ^ 8) : d;
    aoff[di] = 8192 + ((4 * kh + g) * 72 + dp) * 16;
  }
  f32x4 accB[3][2], accS[3][2];
#pragma unroll
  for (int i = 0; i < 3; ++i)
#pragma unroll
    for (int j = 0; j < 2; ++j) { accB[i][j] = f32x4{0.f, 0.f, 0.f, 0.f}; accS[i][j] = f32x4{0.f, 0.f, 0.f, 0.f}; }

  struct Frags { u32x4 a[3]; u32x4 b[2]; };
  auto body = [&](auto DHc) {
    constexpr int DH = decltype(DHc)::value;
    constexpr int NA = DH ? 2 : 3;
    auto rdb = [&](int it, Frags& F) {
      const char* sl = lds + (it & 7) * K3_SLOT;
      F.b[0] = *(const u32x4*)(sl + poff);
      F.b[1] = *(const u32x4*)(sl + poff + 256);
    };
    auto rda = [&](int it, Frags& F) {
      const char* sl = lds + (it & 7) * K3_SLOT;
#pragma unroll
      for (int di = 0; di < NA; ++di) F.a[di] = *(const u32x4*)(sl + aoff[di]);
    };
    auto mm1 = [&](const Frags& F, f32x4 (&acc)[3][2], int di) {
      acc[di][0] = mfma16(F.a[di], F.b[0], acc[di][0]);
      acc[di][1] = mfma16(F.a[di], F.b[1], acc[di][1]);
    };
    auto stage = [&](const Frags& C, Frags& N, f32x4 (&acc)[3][2], int rit, int lit, auto LDc, auto RDc) {
      constexpr bool LD = decltype(LDc)::value, RD = decltype(RDc)::value;
      SB();
      if (RD) rdb(rit, N);
      mm1(C, acc, 0); if (LD) piece(lit, 0); SB();
      if (RD) rda(rit, N);
      mm1(C, acc, 1); if (LD) piece(lit, 1); SB();
      if (NA == 3) mm1(C, acc, 2);
      if (LD) piece(lit, 2);
      SB();
    };
    using LDT = std::true_type;
    using T = std::true_type; using Fa = std::false_type;
    Frags F0, F1;
    K3_WAIT(7, 0); BAR();
    STAMP(ws, 3, 1);
    rdb(0, F0); rda(0, F0);
    for (int it = 0; it < 36; it += 2) {
      K3_WAIT(6, 0); BAR();
      stage(F0, F1, accB, it + 1, it + 8, LDT{}, T{});
      K3_WAIT(6, 0); BAR();
      stage(F1, F0, accB, it + 2, it + 9, LDT{}, T{});
    }
    STAMP(ws, 3, 2);
    K3_WAIT(6, 0); BAR();
    stage(F0, F1, accS, 37, 44, LDT{}, T{});
    const int t = t0 + 32 * th + 16 * kh + l15;
    u32x2 xv[3];
#pragma unroll
    for (int di = 0; di < 3; ++di) {
      int d = 48 * dh + 16 * di + 4 * g;
      d = d < 72 ? d : 0;
      xv[di] = *(const u32x2*)(x + (size_t)t * 576 + d0 + d);
    }
    K3_WAIT(6, 3); BAR(); stage(F1, F0, accS, 38, 0, Fa{}, T{});
    K3_WAIT(5, 3); BAR(); stage(F0, F1, accS, 39, 0, Fa{}, T{});
    K3_WAIT(4, 3); BAR(); stage(F1, F0, accS, 40, 0, Fa{}, T{});
    K3_WAIT(3, 3); BAR(); stage(F0, F1, accS, 41, 0, Fa{}, T{});
    K3_WAIT(2, 3); BAR(); stage(F1, F0, accS, 42, 0, Fa{}, T{});
    K3_WAIT(1, 3); BAR(); stage(F0, F1, accS, 43, 0, Fa{}, T{});
    K3_WAIT(0, 3); BAR(); stage(F1, F0, accS, 44, 0, Fa{}, T{});
    stage(F0, F1, accS, 0, 0, Fa{}, Fa{});
    asm volatile("" ::"v"(xv[0]), "v"(xv[1]), "v"(xv[2]));
    STAMP(ws, 3, 3);
    BAR();
    char* xch = lds + ((th * 2 + dh) * 2) * 6144 + lane * 16;
    auto finish = [&](auto KH) {
      constexpr int own = decltype(KH)::value, oth = own ^ 1;
#pragma unroll
      for (int di = 0; di < NA; ++di) {
        *(f32x4*)(xch + oth * 6144 + di * 1024) = accB[di][oth];
        *(f32x4*)(xch + oth * 6144 + 3072 + di * 1024) = accS[di][oth];
      }
      BAR();
#pragma unroll
      for (int di = 0; di < NA; ++di) {
        f32x4 Bv = accB[di][own] + *(const f32x4*)(xch + own * 6144 + di * 1024);
        f32x4 Sv = accS[di][own] + *(const f32x4*)(xch + own * 6144 + 3072 + di * 1024);
        int d = 48 * dh + 16 * di + 4 * g;
        if (d < 72) {
          u32x2 r;
          r[0] = pk_bf16(bf_lo(xv[di][0]) + Sv[0] * silu_f(Bv[0]), bf_hi(xv[di][0]) + Sv[1] * silu_f(Bv[1]));
          r[1] = pk_bf16(bf_lo(xv[di][1]) + Sv[2] * silu_f(Bv[2]), bf_hi(xv[di][1]) + Sv[3] * silu_f(Bv[3]));
          st64<AUX3>(MAKE_SRD(out), (const char*)out, (const char*)(out + (size_t)t * 576 + d0 + d), r);
        }
      }
    };
    if (kh == 0) finish(std::integral_constant<int, 0>{});
    else         finish(std::integral_constant<int, 1>{});
    STAMP(ws, 3, 4);
  };
  if (dh == 0) body(std::integral_constant<int, 0>{});
  else         body(std::integral_constant<int, 1>{});
}

__global__ __launch_bounds__(512, 2) void k3_out(const u16* __restrict__ x, char* __restrict__ ws, u16* __restrict__ out) {
  __shared__ __attribute__((aligned(16))) char lds[8 * K3_SLOT + 1024];
  CODE_WARM("_Z6k3_outPKtPcPt", 2, 13824, LDS_ADDR(lds) + 8 * K3_SLOT);
  if (__builtin_amdgcn_readfirstlane(threadIdx.x >> 6) == 0) k3_body<1>(lds, x, ws, out);
  else                                                       k3_body<0>(lds, x, ws, out);
}

static inline void launch_stage(int st, void* const* d_in, void* d_out, void* d_ws, hipStream_t stream) {
  const u16* x = (const u16*)d_in[0];
  const u16* wqkv = (const u16*)d_in[1];
  const u16* wo = (const u16*)d_in[2];
  const u16* w_in = (const u16*)d_in[4];
  const u16* w_sp = (const u16*)d_in[5];
  const u16* b_sp = (const u16*)d_in[6];
  const u16* w_out = (const u16*)d_in[7];
  char* ws = (char*)d_ws;
  if (st == 0) k0_inproj<<<256, 512, 0, stream>>>(x, wqkv, w_in, w_sp, w_out, wo, ws);
  if (st == 1) k1_mid<<<256, 512, 0, stream>>>(w_out, wo, ws);
  if (st == 2) k2_spatial<<<256, 512, 0, stream>>>(w_sp, b_sp, ws);
  if (st == 3) k3_out<<<256, 512, 0, stream>>>(x, ws, (u16*)d_out);
}

extern "C" void kernel_launch(void* const* d_in, const int* in_sizes, int n_in, void* d_out, int out_size, void* d_ws,
                              size_t ws_size, hipStream_t stream) {
  (void)in_sizes; (void)n_in; (void)out_size; (void)ws_size;
  for (int st = 0; st < 4; ++st) launch_stage(st, d_in, d_out, d_ws, stream);
}
```
